```python
import torch

def _softmax(x, axis=-1):
    return torch.softmax(x, dim=axis)
def _gelu(x):
    return torch.nn.functional.gelu(x, approximate="tanh")
def _silu(x):
    return torch.nn.functional.silu(x)
def _sigmoid(x):
    return torch.sigmoid(x)
def _layernorm(x, axis=-1, eps=1e-5):
    m = x.mean(dim=axis, keepdim=True)
    v = x.var(dim=axis, keepdim=True, unbiased=False)
    return (x - m) / torch.sqrt(v + eps)
def _scatter_add(tgt, idx, src, axis=0):
    return tgt.clone().index_add_(axis, idx, src)
def _segment_sum(src, seg, n):
    out = torch.zeros((n,) + src.shape[1:], dtype=src.dtype)
    return out.index_add_(0, seg, src)
def _pad(x, pw):
    flat = tuple(v for pair in reversed(pw) for v in pair)
    return torch.nn.functional.pad(x, flat)

kernel_name = 'interleave_layers__gqa_block_cached__gqa_block_cached__decode'

def setup_inputs():

    _w = (lambda a: (lambda t: t.to(torch.bfloat16) if t.is_floating_point() else t)(torch.as_tensor(a)))
    def _A_setup():
        B, Sq, D = 8, 1, 512
        Hq, Hkv, Sk, Dh = 32, 8, 4096, 128
        assert Hq % Hkv == 0, (Hq, Hkv)
        return {
            "x": _w((lambda *s: torch.randn(*s, dtype=torch.bfloat16) * 0.1)(B, Sq, D)),
            "k_cache": _w((lambda *s: torch.randn(*s, dtype=torch.bfloat16) * 0.8)(B, Hkv, Sk, Dh)),
            "v_cache": _w((lambda *s: torch.randn(*s, dtype=torch.bfloat16) * 3.2)(B, Hkv, Sk, Dh)),
            "wq": _w((lambda *s: torch.randn(*s, dtype=torch.bfloat16) * 0.8)(D, Hq * Dh) * (D ** -0.5)),
            "wo": _w((lambda *s: torch.randn(*s, dtype=torch.bfloat16) * 3.2)(Hq * Dh, D) * ((Hq * Dh) ** -0.5)),
        }
    _A_in = _A_setup()

    def _B_setup():
        B, Sq, D = 8, 1, 512
        Hq, Hkv, Sk, Dh = 32, 8, 4096, 128
        assert Hq % Hkv == 0, (Hq, Hkv)
        return {
            "x": _w((lambda *s: torch.randn(*s, dtype=torch.bfloat16) * 0.1)(B, Sq, D)),
            "k_cache": _w((lambda *s: torch.randn(*s, dtype=torch.bfloat16) * 1.6)(B, Hkv, Sk, Dh)),
            "v_cache": _w((lambda *s: torch.randn(*s, dtype=torch.bfloat16) * 1.6)(B, Hkv, Sk, Dh)),
            "wq": _w((lambda *s: torch.randn(*s, dtype=torch.bfloat16) * 1.6)(D, Hq * Dh) * (D ** -0.5)),
            "wo": _w((lambda *s: torch.randn(*s, dtype=torch.bfloat16) * 1.6)(Hq * Dh, D) * ((Hq * Dh) ** -0.5)),
        }
    _B_in = _B_setup()

    return {
        "A_x": _A_in["x"],
        "A_k_cache": _A_in["k_cache"],
        "A_v_cache": _A_in["v_cache"],
        "A_wq": _A_in["wq"],
        "A_wo": _A_in["wo"],
        "B_x": _B_in["x"],
        "B_k_cache": _B_in["k_cache"],
        "B_v_cache": _B_in["v_cache"],
        "B_wq": _B_in["wq"],
        "B_wo": _B_in["wo"],
    }

def reference(A_x, A_k_cache, A_v_cache, A_wq, A_wo, B_x, B_k_cache, B_v_cache, B_wq, B_wo):
    def _A_ref(x, k_cache, v_cache, wq, wo):
        B, Sq, D = x.shape
        Hq, Dh = 32, 128
        Hkv = k_cache.shape[1]
        rep = Hq // Hkv
        q = (lambda x, *d: x.permute(*d))((x @ wq).reshape(B, Sq, Hq, Dh), 0, 2, 1, 3)
        k = (lambda x, n, axis: torch.repeat_interleave(x, n, dim=axis))(k_cache, rep, axis=1)
        v = (lambda x, n, axis: torch.repeat_interleave(x, n, dim=axis))(v_cache, rep, axis=1)
        scores = torch.einsum("bhqd,bhkd->bhqk", q, k) * (Dh ** -0.5)
        p = _softmax(scores, axis=-1)
        o = torch.einsum("bhqk,bhkd->bhqd", p, v)
        return (lambda x, *d: x.permute(*d))(o, 0, 2, 1, 3).reshape(B, Sq, Hq * Dh) @ wo

    def _B_ref(x, k_cache, v_cache, wq, wo):
        B, Sq, D = x.shape
        Hq, Dh = 32, 128
        Hkv = k_cache.shape[1]
        rep = Hq // Hkv
        q = (lambda x, *d: x.permute(*d))((x @ wq).reshape(B, Sq, Hq, Dh), 0, 2, 1, 3)
        k = (lambda x, n, axis: torch.repeat_interleave(x, n, dim=axis))(k_cache, rep, axis=1)
        v = (lambda x, n, axis: torch.repeat_interleave(x, n, dim=axis))(v_cache, rep, axis=1)
        scores = torch.einsum("bhqd,bhkd->bhqk", q, k) * (Dh ** -0.5)
        p = _softmax(scores, axis=-1)
        o = torch.einsum("bhqk,bhkd->bhqd", p, v)
        return (lambda x, *d: x.permute(*d))(o, 0, 2, 1, 3).reshape(B, Sq, Hq * Dh) @ wo

    x = A_x
    x = x + _A_ref(_layernorm(x, axis=-1), A_k_cache, A_v_cache, A_wq, A_wo)
    x = x + _B_ref(_layernorm(x, axis=-1), B_k_cache, B_v_cache, B_wq, B_wo)
    x = x + _A_ref(_layernorm(x, axis=-1), A_k_cache, A_v_cache, A_wq, A_wo)
    x = x + _B_ref(_layernorm(x, axis=-1), B_k_cache, B_v_cache, B_wq, B_wo)
    return x
```

```cpp
#include <hip/hip_runtime.h>
#include <hip/hip_bf16.h>
#include <stdint.h>

typedef unsigned u32x4 __attribute__((ext_vector_type(4)));
typedef float f32x4 __attribute__((ext_vector_type(4)));
typedef short bf16x8 __attribute__((ext_vector_type(8)));
typedef unsigned short u16;

constexpr int B = 8, D = 512, HQ = 32, HKV = 8, SK = 4096, DH = 128;
constexpr int NSPLIT = 8;

__device__ __forceinline__ float bf2f(u16 b) { return __builtin_bit_cast(float, (unsigned)b << 16); }
__device__ __forceinline__ unsigned cvtpk(float lo, float hi) {
  typedef float f2 __attribute__((ext_vector_type(2)));
  typedef __bf16 b2 __attribute__((ext_vector_type(2)));
  f2 v = {lo, hi};
  return __builtin_bit_cast(unsigned, __builtin_convertvector(v, b2));
}
__device__ __forceinline__ float rbf(float x) { return __builtin_bit_cast(float, cvtpk(x, x) << 16); }

__global__ void __launch_bounds__(256) k_q(const u16* __restrict__ x, const u16* __restrict__ wq, u16* __restrict__ q) {
  __shared__ float xn[B][D];
  __shared__ float red[4][B][64];
  const int tid = threadIdx.x, lane = tid & 63, w = tid >> 6;
  for (int b = w; b < B; b += 4) {
    float v[8]; float s = 0.f;
    for (int i = 0; i < 8; ++i) { v[i] = bf2f(x[b * D + lane * 8 + i]); s += v[i]; }
    for (int o = 32; o; o >>= 1) s += __shfl_xor(s, o);
    float m = s * (1.f / D);
    float ss = 0.f;
    for (int i = 0; i < 8; ++i) { float d = v[i] - m; ss += d * d; }
    for (int o = 32; o; o >>= 1) ss += __shfl_xor(ss, o);
    float r = 1.f / sqrtf(ss * (1.f / D) + 1e-5f);
    for (int i = 0; i < 8; ++i) xn[b][lane * 8 + i] = rbf((v[i] - m) * r);
  }
  __syncthreads();
  const int col = blockIdx.x * 64 + lane;
  float acc[B];
  for (int b = 0; b < B; ++b) acc[b] = 0.f;
  for (int k = w * 128; k < w * 128 + 128; ++k) {
    float wv = bf2f(wq[(size_t)k * (HQ * DH) + col]);
    for (int b = 0; b < B; ++b) acc[b] += xn[b][k] * wv;
  }
  for (int b = 0; b < B; ++b) red[w][b][lane] = acc[b];
  __syncthreads();
  for (int i = tid; i < B * 64; i += 256) {
    int b = i >> 6, c = i & 63;
    float s = red[0][b][c] + red[1][b][c] + red[2][b][c] + red[3][b][c];
    q[b * (HQ * DH) + blockIdx.x * 64 + c] = (u16)(cvtpk(s, s) & 0xffff);
  }
}

__global__ void __launch_bounds__(256) k_attn(const u16* __restrict__ q, const u16* __restrict__ kc, const u16* __restrict__ vc,
                                              float* __restrict__ po, float* __restrict__ pml) {
  __shared__ float mrg[4][4][132];
  const int s = blockIdx.x, kvh = blockIdx.y, b = blockIdx.z;
  const int tid = threadIdx.x, lane = tid & 63, w = tid >> 6, g = lane >> 4, m = lane & 15;
  constexpr int KPS = SK / NSPLIT, KPW = KPS / 4;
  const size_t base = ((size_t)(b * HKV + kvh) * SK + s * KPS + w * KPW) * DH;
  bf16x8 qf[4];
  for (int c = 0; c < 4; ++c) qf[c] = *(const bf16x8*)(q + b * (HQ * DH) + (kvh * 4 + (m & 3)) * DH + 32 * c + 8 * g);
  constexpr float C2 = 0.08838834764831845f * 1.4426950408889634f;
  float mrun = -INFINITY, lrun = 0.f;
  f32x4 acc[8];
  for (int j = 0; j < 8; ++j) acc[j] = f32x4{0, 0, 0, 0};
  for (int k0 = 0; k0 < KPW; k0 += 32) {
    const u16* kp = kc + base + (size_t)k0 * DH;
    const u16* vp = vc + base + (size_t)k0 * DH;
    bf16x8 kf[2][4];
    for (int t = 0; t < 2; ++t)
      for (int c = 0; c < 4; ++c)
        kf[t][c] = __builtin_nontemporal_load((const bf16x8*)(kp + (16 * t + 4 * (m & 3) + (m >> 2)) * DH + 32 * c + 8 * g));
    u32x4 vr[8];
    for (int i = 0; i < 8; ++i) vr[i] = __builtin_nontemporal_load((const u32x4*)(vp + (4 * i + g) * DH + 8 * m));
    f32x4 sc[2];
    for (int t = 0; t < 2; ++t) {
      sc[t] = f32x4{0, 0, 0, 0};
      for (int c = 0; c < 4; ++c) sc[t] = __builtin_amdgcn_mfma_f32_16x16x32_bf16(kf[t][c], qf[c], sc[t], 0, 0, 0);
    }
    float mx = -INFINITY;
    for (int t = 0; t < 2; ++t) for (int r = 0; r < 4; ++r) { sc[t][r] *= C2; mx = fmaxf(mx, sc[t][r]); }
    mx = fmaxf(mx, __shfl_xor(mx, 16)); mx = fmaxf(mx, __shfl_xor(mx, 32));
    float mn = fmaxf(mrun, mx);
    float alpha = __builtin_amdgcn_exp2f(mrun - mn);
    mrun = mn;
    float p[8]; float ps = 0.f;
    for (int t = 0; t < 2; ++t) for (int r = 0; r < 4; ++r) { p[4 * t + r] = __builtin_amdgcn_exp2f(sc[t][r] - mn); }
    u32x4 pf = {cvtpk(p[0], p[1]), cvtpk(p[2], p[3]), cvtpk(p[4], p[5]), cvtpk(p[6], p[7])};
    for (int i = 0; i < 4; ++i) ps += __builtin_bit_cast(float, pf[i] << 16) + __builtin_bit_cast(float, pf[i] & 0xffff0000u);
    lrun = lrun * alpha + ps;
    float al[4];
    for (int r = 0; r < 4; ++r) al[r] = __shfl(alpha, r);
    for (int j = 0; j < 8; ++j) {
      u32x4 vf;
      for (int ii = 0; ii < 4; ++ii) {
        unsigned a = vr[2 * ii][j >> 1], bb = vr[2 * ii + 1][j >> 1];
        vf[ii] = (j & 1) ? __builtin_amdgcn_perm(bb, a, 0x07060302u) : __builtin_amdgcn_perm(bb, a, 0x05040100u);
      }
      for (int r = 0; r < 4; ++r) acc[j][r] *= al[r];
      acc[j] = __builtin_amdgcn_mfma_f32_16x16x32_bf16(__builtin_bit_cast(bf16x8, pf), __builtin_bit_cast(bf16x8, vf), acc[j], 0, 0, 0);
    }
  }
  lrun += __shfl_xor(lrun, 16); lrun += __shfl_xor(lrun, 32);
  if (g == 0) {
    for (int j = 0; j < 8; ++j) for (int r = 0; r < 4; ++r) mrg[w][r][4 + 8 * m + j] = acc[j][r];
    if (m < 4) { mrg[w][m][0] = mrun; mrg[w][m][1] = lrun; }
  }
  __syncthreads();
  {
    const int h = w;
    float M = fmaxf(fmaxf(mrg[0][h][0], mrg[1][h][0]), fmaxf(mrg[2][h][0], mrg[3][h][0]));
    float L = 0.f, o0 = 0.f, o1 = 0.f;
    for (int ww = 0; ww < 4; ++ww) {
      float wt = __builtin_amdgcn_exp2f(mrg[ww][h][0] - M);
      L += wt * mrg[ww][h][1]; o0 += wt * mrg[ww][h][4 + 2 * lane]; o1 += wt * mrg[ww][h][5 + 2 * lane];
    }
    size_t slot = (size_t)(b * HQ + kvh * 4 + h) * NSPLIT + s;
    po[slot * DH + 2 * lane] = o0; po[slot * DH + 2 * lane + 1] = o1;
    if (lane == 0) { pml[2 * slot] = M; pml[2 * slot + 1] = L; }
  }
}

__global__ void __launch_bounds__(64) k_comb(const float* __restrict__ po, const float* __restrict__ pml, float* __restrict__ o) {
  const int row = blockIdx.x, lane = threadIdx.x;
  float M = -INFINITY;
  for (int s = 0; s < NSPLIT; ++s) M = fmaxf(M, pml[2 * (row * NSPLIT + s)]);
  float L = 0.f, o0 = 0.f, o1 = 0.f;
  for (int s = 0; s < NSPLIT; ++s) {
    float wt = __builtin_amdgcn_exp2f(pml[2 * (row * NSPLIT + s)] - M);
    L += wt * pml[2 * (row * NSPLIT + s) + 1];
    o0 += wt * po[(size_t)(row * NSPLIT + s) * DH + 2 * lane]; o1 += wt * po[(size_t)(row * NSPLIT + s) * DH + 2 * lane + 1];
  }
  o[row * DH + 2 * lane] = rbf(o0 / L); o[row * DH + 2 * lane + 1] = rbf(o1 / L);
}

__global__ void __launch_bounds__(256) k_o(const float* __restrict__ o, const u16* __restrict__ wo, float* __restrict__ part) {
  __shared__ float os[B][128];
  __shared__ float red[4][B][64];
  const int tid = threadIdx.x, lane = tid & 63, w = tid >> 6;
  const int cb = blockIdx.x, ks = blockIdx.y;
  for (int i = tid; i < B * 128; i += 256) os[i >> 7][i & 127] = o[(i >> 7) * (HQ * DH) + ks * 128 + (i & 127)];
  __syncthreads();
  float acc[B];
  for (int b = 0; b < B; ++b) acc[b] = 0.f;
  for (int k = w * 32; k < w * 32 + 32; ++k) {
    float wv = bf2f(wo[(size_t)(ks * 128 + k) * D + cb * 64 + lane]);
    for (int b = 0; b < B; ++b) acc[b] += os[b][k] * wv;
  }
  for (int b = 0; b < B; ++b) red[w][b][lane] = acc[b];
  __syncthreads();
  for (int i = tid; i < B * 64; i += 256) {
    int b = i >> 6, c = i & 63;
    part[(size_t)ks * (B * D) + b * D + cb * 64 + c] = red[0][b][c] + red[1][b][c] + red[2][b][c] + red[3][b][c];
  }
}

__global__ void __launch_bounds__(256) k_res(const float* __restrict__ part, const u16* __restrict__ xin, u16* __restrict__ xout) {
  const int i = blockIdx.x * 256 + threadIdx.x;
  float s = 0.f;
  for (int ks = 0; ks < 32; ++ks) s += part[(size_t)ks * (B * D) + i];
  float r = bf2f(xin[i]) + rbf(s);
  xout[i] = (u16)(cvtpk(r, r) & 0xffff);
}

extern "C" void kernel_launch(void* const* d_in, const int* in_sizes, int n_in,
                              void* d_out, int out_size, void* d_ws, size_t ws_size,
                              hipStream_t stream) {
  char* ws = (char*)d_ws;
  u16* q = (u16*)ws;
  float* po = (float*)(ws + (1 << 20));
  float* pml = (float*)(ws + (3 << 20));
  float* o = (float*)(ws + (4 << 20));
  float* part = (float*)(ws + (5 << 20));
  u16* xb[2] = {(u16*)(ws + (6 << 20)), (u16*)(ws + (7 << 20))};
  const u16* xin = (const u16*)d_in[0];
  for (int l = 0; l < 4; ++l) {
    const int o5 = (l & 1) * 5;
    const u16* kc = (const u16*)d_in[o5 + 1];
    const u16* vc = (const u16*)d_in[o5 + 2];
    const u16* wq = (const u16*)d_in[o5 + 3];
    const u16* wo = (const u16*)d_in[o5 + 4];
    u16* xout = (l == 3) ? (u16*)d_out : xb[l & 1];
    k_q<<<64, 256, 0, stream>>>(xin, wq, q);
    k_attn<<<dim3(NSPLIT, HKV, B), 256, 0, stream>>>(q, kc, vc, po, pml);
    k_comb<<<B * HQ, 64, 0, stream>>>(po, pml, o);
    k_o<<<dim3(8, 32), 256, 0, stream>>>(o, wo, part);
    k_res<<<16, 256, 0, stream>>>(part, xin, xout);
    xin = xout;
  }
}
```

```cpp
#include <hip/hip_runtime.h>
#include <hip/hip_bf16.h>
#include <stdint.h>

typedef unsigned u32x4 __attribute__((ext_vector_type(4)));
typedef float f32x4 __attribute__((ext_vector_type(4)));
typedef short bf16x8 __attribute__((ext_vector_type(8)));
typedef unsigned short u16;

constexpr int B = 8, D = 512, HQ = 32, HKV = 8, SK = 4096, DH = 128;
constexpr int NSPLIT = 8;
constexpr int POL = 15;

__device__ __forceinline__ float bf2f(u16 b) { return __builtin_bit_cast(float, (unsigned)b << 16); }
__device__ __forceinline__ unsigned cvtpk(float lo, float hi) {
  typedef float f2 __attribute__((ext_vector_type(2)));
  typedef __bf16 b2 __attribute__((ext_vector_type(2)));
  f2 v = {lo, hi};
  return __builtin_bit_cast(unsigned, __builtin_convertvector(v, b2));
}
__device__ __forceinline__ float rbf(float x) { return __builtin_bit_cast(float, cvtpk(x, x) << 16); }

__global__ void __launch_bounds__(256) k_q(const u16* __restrict__ x, const u16* __restrict__ wq, u16* __restrict__ q) {
  __shared__ float xn[B][D];
  __shared__ float red[4][B][64];
  const int tid = threadIdx.x, lane = tid & 63, w = tid >> 6;
  for (int b = w; b < B; b += 4) {
    float v[8]; float s = 0.f;
    for (int i = 0; i < 8; ++i) { v[i] = bf2f(x[b * D + lane * 8 + i]); s += v[i]; }
    for (int o = 32; o; o >>= 1) s += __shfl_xor(s, o);
    float m = s * (1.f / D);
    float ss = 0.f;
    for (int i = 0; i < 8; ++i) { float d = v[i] - m; ss += d * d; }
    for (int o = 32; o; o >>= 1) ss += __shfl_xor(ss, o);
    float r = 1.f / sqrtf(ss * (1.f / D) + 1e-5f);
    for (int i = 0; i < 8; ++i) xn[b][lane * 8 + i] = rbf((v[i] - m) * r);
  }
  __syncthreads();
  const int col = blockIdx.x * 64 + lane;
  float acc[B];
  for (int b = 0; b < B; ++b) acc[b] = 0.f;
  for (int k = w * 128; k < w * 128 + 128; ++k) {
    float wv = bf2f(wq[(size_t)k * (HQ * DH) + col]);
    for (int b = 0; b < B; ++b) acc[b] += xn[b][k] * wv;
  }
  for (int b = 0; b < B; ++b) red[w][b][lane] = acc[b];
  __syncthreads();
  for (int i = tid; i < B * 64; i += 256) {
    int b = i >> 6, c = i & 63;
    float s = red[0][b][c] + red[1][b][c] + red[2][b][c] + red[3][b][c];
    q[b * (HQ * DH) + blockIdx.x * 64 + c] = (u16)(cvtpk(s, s) & 0xffff);
  }
}

template <int NT> __global__ void __launch_bounds__(256) k_attn(const u16* __restrict__ q, const u16* __restrict__ kc, const u16* __restrict__ vc,
                                              float* __restrict__ po, float* __restrict__ pml) {
  __shared__ float mrg[4][4][132];
  const int s = blockIdx.x, kvh = blockIdx.y, b = blockIdx.z;
  const int tid = threadIdx.x, lane = tid & 63, w = tid >> 6, g = lane >> 4, m = lane & 15;
  constexpr int KPS = SK / NSPLIT, KPW = KPS / 4;
  const size_t base = ((size_t)(b * HKV + kvh) * SK + s * KPS + w * KPW) * DH;
  bf16x8 qf[4];
  for (int c = 0; c < 4; ++c) qf[c] = *(const bf16x8*)(q + b * (HQ * DH) + (kvh * 4 + (m & 3)) * DH + 32 * c + 8 * g);
  constexpr float C2 = 0.08838834764831845f * 1.4426950408889634f;
  float mrun = -INFINITY, lrun = 0.f;
  f32x4 acc[8];
  for (int j = 0; j < 8; ++j) acc[j] = f32x4{0, 0, 0, 0};
  for (int k0 = 0; k0 < KPW; k0 += 32) {
    const u16* kp = kc + base + (size_t)k0 * DH;
    const u16* vp = vc + base + (size_t)k0 * DH;
    bf16x8 kf[2][4];
    for (int t = 0; t < 2; ++t)
      for (int c = 0; c < 4; ++c)
        { const bf16x8* pp = (const bf16x8*)(kp + (16 * t + 4 * (m & 3) + (m >> 2)) * DH + 32 * c + 8 * g); kf[t][c] = NT ? __builtin_nontemporal_load(pp) : *pp; }
    u32x4 vr[8];
    for (int i = 0; i < 8; ++i) { const u32x4* pp = (const u32x4*)(vp + (4 * i + g) * DH + 8 * m); vr[i] = NT ? __builtin_nontemporal_load(pp) : *pp; }
    f32x4 sc[2];
    for (int t = 0; t < 2; ++t) {
      sc[t] = f32x4{0, 0, 0, 0};
      for (int c = 0; c < 4; ++c) sc[t] = __builtin_amdgcn_mfma_f32_16x16x32_bf16(kf[t][c], qf[c], sc[t], 0, 0, 0);
    }
    float mx = -INFINITY;
    for (int t = 0; t < 2; ++t) for (int r = 0; r < 4; ++r) { sc[t][r] *= C2; mx = fmaxf(mx, sc[t][r]); }
    mx = fmaxf(mx, __shfl_xor(mx, 16)); mx = fmaxf(mx, __shfl_xor(mx, 32));
    float mn = fmaxf(mrun, mx);
    float alpha = __builtin_amdgcn_exp2f(mrun - mn);
    mrun = mn;
    float p[8]; float ps = 0.f;
    for (int t = 0; t < 2; ++t) for (int r = 0; r < 4; ++r) { p[4 * t + r] = __builtin_amdgcn_exp2f(sc[t][r] - mn); }
    u32x4 pf = {cvtpk(p[0], p[1]), cvtpk(p[2], p[3]), cvtpk(p[4], p[5]), cvtpk(p[6], p[7])};
    for (int i = 0; i < 4; ++i) ps += __builtin_bit_cast(float, pf[i] << 16) + __builtin_bit_cast(float, pf[i] & 0xffff0000u);
    lrun = lrun * alpha + ps;
    float al[4];
    for (int r = 0; r < 4; ++r) al[r] = __shfl(alpha, r);
    for (int j = 0; j < 8; ++j) {
      u32x4 vf;
      for (int ii = 0; ii < 4; ++ii) {
        unsigned a = vr[2 * ii][j >> 1], bb = vr[2 * ii + 1][j >> 1];
        vf[ii] = (j & 1) ? __builtin_amdgcn_perm(bb, a, 0x07060302u) : __builtin_amdgcn_perm(bb, a, 0x05040100u);
      }
      for (int r = 0; r < 4; ++r) acc[j][r] *= al[r];
      acc[j] = __builtin_amdgcn_mfma_f32_16x16x32_bf16(__builtin_bit_cast(bf16x8, pf), __builtin_bit_cast(bf16x8, vf), acc[j], 0, 0, 0);
    }
  }
  lrun += __shfl_xor(lrun, 16); lrun += __shfl_xor(lrun, 32);
  if (g == 0) {
    for (int j = 0; j < 8; ++j) for (int r = 0; r < 4; ++r) mrg[w][r][4 + 8 * m + j] = acc[j][r];
    if (m < 4) { mrg[w][m][0] = mrun; mrg[w][m][1] = lrun; }
  }
  __syncthreads();
  {
    const int h = w;
    float M = fmaxf(fmaxf(mrg[0][h][0], mrg[1][h][0]), fmaxf(mrg[2][h][0], mrg[3][h][0]));
    float L = 0.f, o0 = 0.f, o1 = 0.f;
    for (int ww = 0; ww < 4; ++ww) {
      float wt = __builtin_amdgcn_exp2f(mrg[ww][h][0] - M);
      L += wt * mrg[ww][h][1]; o0 += wt * mrg[ww][h][4 + 2 * lane]; o1 += wt * mrg[ww][h][5 + 2 * lane];
    }
    size_t slot = (size_t)(b * HQ + kvh * 4 + h) * NSPLIT + s;
    po[slot * DH + 2 * lane] = o0; po[slot * DH + 2 * lane + 1] = o1;
    if (lane == 0) { pml[2 * slot] = M; pml[2 * slot + 1] = L; }
  }
}

__global__ void __launch_bounds__(64) k_comb(const float* __restrict__ po, const float* __restrict__ pml, float* __restrict__ o) {
  const int row = blockIdx.x, lane = threadIdx.x;
  float M = -INFINITY;
  for (int s = 0; s < NSPLIT; ++s) M = fmaxf(M, pml[2 * (row * NSPLIT + s)]);
  float L = 0.f, o0 = 0.f, o1 = 0.f;
  for (int s = 0; s < NSPLIT; ++s) {
    float wt = __builtin_amdgcn_exp2f(pml[2 * (row * NSPLIT + s)] - M);
    L += wt * pml[2 * (row * NSPLIT + s) + 1];
    o0 += wt * po[(size_t)(row * NSPLIT + s) * DH + 2 * lane]; o1 += wt * po[(size_t)(row * NSPLIT + s) * DH + 2 * lane + 1];
  }
  o[row * DH + 2 * lane] = rbf(o0 / L); o[row * DH + 2 * lane + 1] = rbf(o1 / L);
}

__global__ void __launch_bounds__(256) k_o(const float* __restrict__ o, const u16* __restrict__ wo, float* __restrict__ part) {
  __shared__ float os[B][128];
  __shared__ float red[4][B][64];
  const int tid = threadIdx.x, lane = tid & 63, w = tid >> 6;
  const int cb = blockIdx.x, ks = blockIdx.y;
  for (int i = tid; i < B * 128; i += 256) os[i >> 7][i & 127] = o[(i >> 7) * (HQ * DH) + ks * 128 + (i & 127)];
  __syncthreads();
  float acc[B];
  for (int b = 0; b < B; ++b) acc[b] = 0.f;
  for (int k = w * 32; k < w * 32 + 32; ++k) {
    float wv = bf2f(wo[(size_t)(ks * 128 + k) * D + cb * 64 + lane]);
    for (int b = 0; b < B; ++b) acc[b] += os[b][k] * wv;
  }
  for (int b = 0; b < B; ++b) red[w][b][lane] = acc[b];
  __syncthreads();
  for (int i = tid; i < B * 64; i += 256) {
    int b = i >> 6, c = i & 63;
    part[(size_t)ks * (B * D) + b * D + cb * 64 + c] = red[0][b][c] + red[1][b][c] + red[2][b][c] + red[3][b][c];
  }
}

__global__ void __launch_bounds__(256) k_res(const float* __restrict__ part, const u16* __restrict__ xin, u16* __restrict__ xout) {
  const int i = blockIdx.x * 256 + threadIdx.x;
  float s = 0.f;
  for (int ks = 0; ks < 32; ++ks) s += part[(size_t)ks * (B * D) + i];
  float r = bf2f(xin[i]) + rbf(s);
  xout[i] = (u16)(cvtpk(r, r) & 0xffff);
}

extern "C" void kernel_launch(void* const* d_in, const int* in_sizes, int n_in,
                              void* d_out, int out_size, void* d_ws, size_t ws_size,
                              hipStream_t stream) {
  char* ws = (char*)d_ws;
  u16* q = (u16*)ws;
  float* po = (float*)(ws + (1 << 20));
  float* pml = (float*)(ws + (3 << 20));
  float* o = (float*)(ws + (4 << 20));
  float* part = (float*)(ws + (5 << 20));
  u16* xb[2] = {(u16*)(ws + (6 << 20)), (u16*)(ws + (7 << 20))};
  const u16* xin = (const u16*)d_in[0];
  for (int l = 0; l < 4; ++l) {
    const int o5 = (l & 1) * 5;
    const u16* kc = (const u16*)d_in[o5 + 1];
    const u16* vc = (const u16*)d_in[o5 + 2];
    const u16* wq = (const u16*)d_in[o5 + 3];
    const u16* wo = (const u16*)d_in[o5 + 4];
    u16* xout = (l == 3) ? (u16*)d_out : xb[l & 1];
    k_q<<<64, 256, 0, stream>>>(xin, wq, q);
    if ((POL >> l) & 1) k_attn<1><<<dim3(NSPLIT, HKV, B), 256, 0, stream>>>(q, kc, vc, po, pml); else k_attn<0><<<dim3(NSPLIT, HKV, B), 256, 0, stream>>>(q, kc, vc, po, pml);
    k_comb<<<B * HQ, 64, 0, stream>>>(po, pml, o);
    k_o<<<dim3(8, 32), 256, 0, stream>>>(o, wo, part);
    k_res<<<16, 256, 0, stream>>>(part, xin, xout);
    xin = xout;
  }
}
```

```cpp
#include <hip/hip_runtime.h>
#include <hip/hip_bf16.h>
#include <stdint.h>

typedef unsigned u32x4 __attribute__((ext_vector_type(4)));
typedef float f32x4 __attribute__((ext_vector_type(4)));
typedef short bf16x8 __attribute__((ext_vector_type(8)));
typedef unsigned short u16;

constexpr int B = 8, D = 512, HQ = 32, HKV = 8, SK = 4096, DH = 128;
constexpr int NSPLIT = 8;
constexpr int POL = 15;

__device__ __forceinline__ float bf2f(u16 b) { return __builtin_bit_cast(float, (unsigned)b << 16); }
__device__ __forceinline__ unsigned cvtpk(float lo, float hi) {
  typedef float f2 __attribute__((ext_vector_type(2)));
  typedef __bf16 b2 __attribute__((ext_vector_type(2)));
  f2 v = {lo, hi};
  return __builtin_bit_cast(unsigned, __builtin_convertvector(v, b2));
}
__device__ __forceinline__ float rbf(float x) { return __builtin_bit_cast(float, cvtpk(x, x) << 16); }

__global__ void __launch_bounds__(256) k_q(const u16* __restrict__ x, const u16* __restrict__ wq, u16* __restrict__ q) {
  __shared__ float xn[B][D];
  __shared__ float red[4][B][64];
  const int tid = threadIdx.x, lane = tid & 63, w = tid >> 6;
  for (int b = w; b < B; b += 4) {
    float v[8]; float s = 0.f;
    for (int i = 0; i < 8; ++i) { v[i] = bf2f(x[b * D + lane * 8 + i]); s += v[i]; }
    for (int o = 32; o; o >>= 1) s += __shfl_xor(s, o);
    float m = s * (1.f / D);
    float ss = 0.f;
    for (int i = 0; i < 8; ++i) { float d = v[i] - m; ss += d * d; }
    for (int o = 32; o; o >>= 1) ss += __shfl_xor(ss, o);
    float r = 1.f / sqrtf(ss * (1.f / D) + 1e-5f);
    for (int i = 0; i < 8; ++i) xn[b][lane * 8 + i] = rbf((v[i] - m) * r);
  }
  __syncthreads();
  const int col = blockIdx.x * 64 + lane;
  float acc[B];
  for (int b = 0; b < B; ++b) acc[b] = 0.f;
  for (int k = w * 128; k < w * 128 + 128; ++k) {
    float wv = bf2f(wq[(size_t)k * (HQ * DH) + col]);
    for (int b = 0; b < B; ++b) acc[b] += xn[b][k] * wv;
  }
  for (int b = 0; b < B; ++b) red[w][b][lane] = acc[b];
  __syncthreads();
  for (int i = tid; i < B * 64; i += 256) {
    int b = i >> 6, c = i & 63;
    float s = red[0][b][c] + red[1][b][c] + red[2][b][c] + red[3][b][c];
    q[b * (HQ * DH) + blockIdx.x * 64 + c] = (u16)(cvtpk(s, s) & 0xffff);
  }
}

template <int NT> __global__ void __launch_bounds__(256) k_attn(const u16* __restrict__ q, const u16* __restrict__ kc, const u16* __restrict__ vc,
                                              float* __restrict__ po, float* __restrict__ pml) {
  __shared__ float mrg[4][4][132];
  const int s = blockIdx.x, kvh = blockIdx.y, b = blockIdx.z;
  const int tid = threadIdx.x, lane = tid & 63, w = tid >> 6, g = lane >> 4, m = lane & 15;
  constexpr int KPS = SK / NSPLIT, KPW = KPS / 4;
  const size_t base = ((size_t)(b * HKV + kvh) * SK + s * KPS + w * KPW) * DH;
  bf16x8 qf[4];
  for (int c = 0; c < 4; ++c) qf[c] = *(const bf16x8*)(q + b * (HQ * DH) + (kvh * 4 + (m & 3)) * DH + 32 * c + 8 * g);
  constexpr float C2 = 0.08838834764831845f * 1.4426950408889634f;
  float mrun = -INFINITY, lrun = 0.f;
  f32x4 acc[8];
  for (int j = 0; j < 8; ++j) acc[j] = f32x4{0, 0, 0, 0};
  for (int k0 = 0; k0 < KPW; k0 += 32) {
    const u16* kp = kc + base + (size_t)k0 * DH;
    const u16* vp = vc + base + (size_t)k0 * DH;
    bf16x8 kf[2][4];
    for (int t = 0; t < 2; ++t)
      for (int c = 0; c < 4; ++c)
        { const bf16x8* pp = (const bf16x8*)(kp + (16 * t + 4 * (m & 3) + (m >> 2)) * DH + 32 * c + 8 * g); kf[t][c] = NT ? __builtin_nontemporal_load(pp) : *pp; }
    u32x4 vr[8];
    for (int i = 0; i < 8; ++i) { const u32x4* pp = (const u32x4*)(vp + (4 * i + g) * DH + 8 * m); vr[i] = NT ? __builtin_nontemporal_load(pp) : *pp; }
    f32x4 sc[2];
    for (int t = 0; t < 2; ++t) {
      sc[t] = f32x4{0, 0, 0, 0};
      for (int c = 0; c < 4; ++c) sc[t] = __builtin_amdgcn_mfma_f32_16x16x32_bf16(kf[t][c], qf[c], sc[t], 0, 0, 0);
    }
    float mx = -INFINITY;
    for (int t = 0; t < 2; ++t) for (int r = 0; r < 4; ++r) { sc[t][r] *= C2; mx = fmaxf(mx, sc[t][r]); }
    mx = fmaxf(mx, __shfl_xor(mx, 16)); mx = fmaxf(mx, __shfl_xor(mx, 32));
    float mn = fmaxf(mrun, mx);
    float alpha = __builtin_amdgcn_exp2f(mrun - mn);
    mrun = mn;
    float p[8]; float ps = 0.f;
    for (int t = 0; t < 2; ++t) for (int r = 0; r < 4; ++r) { p[4 * t + r] = __builtin_amdgcn_exp2f(sc[t][r] - mn); }
    u32x4 pf = {cvtpk(p[0], p[1]), cvtpk(p[2], p[3]), cvtpk(p[4], p[5]), cvtpk(p[6], p[7])};
    for (int i = 0; i < 4; ++i) ps += __builtin_bit_cast(float, pf[i] << 16) + __builtin_bit_cast(float, pf[i] & 0xffff0000u);
    lrun = lrun * alpha + ps;
    float al[4];
    for (int r = 0; r < 4; ++r) al[r] = __shfl(alpha, r);
    for (int j = 0; j < 8; ++j) {
      u32x4 vf;
      for (int ii = 0; ii < 4; ++ii) {
        unsigned a = vr[2 * ii][j >> 1], bb = vr[2 * ii + 1][j >> 1];
        vf[ii] = (j & 1) ? __builtin_amdgcn_perm(bb, a, 0x07060302u) : __builtin_amdgcn_perm(bb, a, 0x05040100u);
      }
      for (int r = 0; r < 4; ++r) acc[j][r] *= al[r];
      acc[j] = __builtin_amdgcn_mfma_f32_16x16x32_bf16(__builtin_bit_cast(bf16x8, pf), __builtin_bit_cast(bf16x8, vf), acc[j], 0, 0, 0);
    }
  }
  lrun += __shfl_xor(lrun, 16); lrun += __shfl_xor(lrun, 32);
  if (g == 0) {
    for (int j = 0; j < 8; ++j) for (int r = 0; r < 4; ++r) mrg[w][r][4 + 8 * m + j] = acc[j][r];
    if (m < 4) { mrg[w][m][0] = mrun; mrg[w][m][1] = lrun; }
  }
  __syncthreads();
  {
    const int h = w;
    float M = fmaxf(fmaxf(mrg[0][h][0], mrg[1][h][0]), fmaxf(mrg[2][h][0], mrg[3][h][0]));
    float L = 0.f, o0 = 0.f, o1 = 0.f;
    for (int ww = 0; ww < 4; ++ww) {
      float wt = __builtin_amdgcn_exp2f(mrg[ww][h][0] - M);
      L += wt * mrg[ww][h][1]; o0 += wt * mrg[ww][h][4 + 2 * lane]; o1 += wt * mrg[ww][h][5 + 2 * lane];
    }
    size_t slot = (size_t)(b * HQ + kvh * 4 + h) * NSPLIT + s;
    po[slot * DH + 2 * lane] = o0; po[slot * DH + 2 * lane + 1] = o1;
    if (lane == 0) { pml[2 * slot] = M; pml[2 * slot + 1] = L; }
  }
}

__global__ void __launch_bounds__(64) k_comb(const float* __restrict__ po, const float* __restrict__ pml, float* __restrict__ o) {
  const int row = blockIdx.x, lane = threadIdx.x;
  float M = -INFINITY;
  for (int s = 0; s < NSPLIT; ++s) M = fmaxf(M, pml[2 * (row * NSPLIT + s)]);
  float L = 0.f, o0 = 0.f, o1 = 0.f;
  for (int s = 0; s < NSPLIT; ++s) {
    float wt = __builtin_amdgcn_exp2f(pml[2 * (row * NSPLIT + s)] - M);
    L += wt * pml[2 * (row * NSPLIT + s) + 1];
    o0 += wt * po[(size_t)(row * NSPLIT + s) * DH + 2 * lane]; o1 += wt * po[(size_t)(row * NSPLIT + s) * DH + 2 * lane + 1];
  }
  o[row * DH + 2 * lane] = rbf(o0 / L); o[row * DH + 2 * lane + 1] = rbf(o1 / L);
}

__global__ void __launch_bounds__(256) k_o(const float* __restrict__ o, const u16* __restrict__ wo, float* __restrict__ part) {
  __shared__ float os[B][128];
  __shared__ float red[4][B][64];
  const int tid = threadIdx.x, lane = tid & 63, w = tid >> 6;
  const int cb = blockIdx.x, ks = blockIdx.y;
  for (int i = tid; i < B * 128; i += 256) os[i >> 7][i & 127] = o[(i >> 7) * (HQ * DH) + ks * 128 + (i & 127)];
  __syncthreads();
  float acc[B];
  for (int b = 0; b < B; ++b) acc[b] = 0.f;
  for (int k = w * 32; k < w * 32 + 32; ++k) {
    float wv = bf2f(wo[(size_t)(ks * 128 + k) * D + cb * 64 + lane]);
    for (int b = 0; b < B; ++b) acc[b] += os[b][k] * wv;
  }
  for (int b = 0; b < B; ++b) red[w][b][lane] = acc[b];
  __syncthreads();
  for (int i = tid; i < B * 64; i += 256) {
    int b = i >> 6, c = i & 63;
    part[(size_t)ks * (B * D) + b * D + cb * 64 + c] = red[0][b][c] + red[1][b][c] + red[2][b][c] + red[3][b][c];
  }
}

__global__ void __launch_bounds__(256) k_res(const float* __restrict__ part, const u16* __restrict__ xin, u16* __restrict__ xout) {
  const int i = blockIdx.x * 256 + threadIdx.x;
  float s = 0.f;
  for (int ks = 0; ks < 32; ++ks) s += part[(size_t)ks * (B * D) + i];
  float r = bf2f(xin[i]) + rbf(s);
  xout[i] = (u16)(cvtpk(r, r) & 0xffff);
}


#define GAS __attribute__((address_space(1)))
template <int NT> __device__ __forceinline__ u32x4 ldp(const char* p) {
  if (NT) return __builtin_nontemporal_load((const GAS u32x4*)p);
  else return *(const GAS u32x4*)p;
}
template <int NT, int DEPTH> __device__ __forceinline__ void stream(const char* base, int nbytes, int lane, u32x4& acc) {
  u32x4 r[DEPTH];
  const int n = nbytes / 1024;
#pragma unroll
  for (int i = 0; i < DEPTH; ++i) r[i] = ldp<NT>(base + i * 1024 + lane * 16);
  for (int j = DEPTH; j < n; j += DEPTH) {
    const char* b2 = base + (size_t)j * 1024;
#pragma unroll
    for (int i = 0; i < DEPTH; ++i) { acc ^= r[i]; r[i] = ldp<NT>(b2 + i * 1024 + lane * 16); __builtin_amdgcn_sched_barrier(0); }
  }
#pragma unroll
  for (int i = 0; i < DEPTH; ++i) acc ^= r[i];
  __builtin_amdgcn_sched_barrier(0);
}
template <int NT1, int NT2, int NT3, int FRAC>
__global__ void __launch_bounds__(512) probe(const char* t0, const char* t1, const char* t2, const char* t3, unsigned* sink) {
  __shared__ char smem[96 * 1024];
  const int lane = threadIdx.x & 63, w = __builtin_amdgcn_readfirstlane(threadIdx.x >> 6), c = blockIdx.x;
  constexpr int SL = 256 * 1024, WS = SL / 8;
  u32x4 acc = {0, 0, 0, 0};
  unsigned long long ts = __builtin_amdgcn_s_memrealtime();
  constexpr int keep = WS * FRAC / 16;
#pragma unroll 1
  for (int i = 0; i < 4; ++i) {
    const char* base = (i == 0 ? t0 : i == 1 ? t1 : i == 2 ? t2 : t3) + (size_t)c * SL + w * WS;
    if (WS - keep > 0) stream<NT1, 16>(base, WS - keep, lane, acc);
    if (keep > 0) stream<NT2, 16>(base + WS - keep, keep, lane, acc);
  }
  if (NT3 >= 0) {
#pragma unroll 1
  for (int i = 0; i < 4; ++i) {
    const char* base = (i == 0 ? t0 : i == 1 ? t1 : i == 2 ? t2 : t3) + (size_t)c * SL + w * WS;
    if (keep > 0) stream<NT3, 16>(base + WS - keep, keep, lane, acc);
  }
  }
  if ((acc[0] ^ acc[1] ^ acc[2] ^ acc[3]) == 0x12345678u) { sink[threadIdx.x] = 1; smem[threadIdx.x] = 1; sink[1024] = smem[(threadIdx.x * 7) & 1023]; }
  __syncthreads();
  unsigned long long te = __builtin_amdgcn_s_memrealtime();
  unsigned long long until = ts + (te - ts) * 10;
  while (__builtin_amdgcn_s_memrealtime() < until) __builtin_amdgcn_s_sleep(4);
}

extern "C" void kernel_launch(void* const* d_in, const int* in_sizes, int n_in,
                              void* d_out, int out_size, void* d_ws, size_t ws_size,
                              hipStream_t stream) {
  char* ws = (char*)d_ws;
  u16* q = (u16*)ws;
  float* po = (float*)(ws + (1 << 20));
  float* pml = (float*)(ws + (3 << 20));
  float* o = (float*)(ws + (4 << 20));
  float* part = (float*)(ws + (5 << 20));
  u16* xb[2] = {(u16*)(ws + (6 << 20)), (u16*)(ws + (7 << 20))};
  probe<1,1,-1,16><<<256, 512, 0, stream>>>((const char*)d_in[1], (const char*)d_in[2], (const char*)d_in[6], (const char*)d_in[7], (unsigned*)(ws + (16 << 20)));
  const u16* xin = (const u16*)d_in[0];
  for (int l = 0; l < 4; ++l) {
    const int o5 = (l & 1) * 5;
    const u16* kc = (const u16*)d_in[o5 + 1];
    const u16* vc = (const u16*)d_in[o5 + 2];
    const u16* wq = (const u16*)d_in[o5 + 3];
    const u16* wo = (const u16*)d_in[o5 + 4];
    u16* xout = (l == 3) ? (u16*)d_out : xb[l & 1];
    k_q<<<64, 256, 0, stream>>>(xin, wq, q);
    if ((POL >> l) & 1) k_attn<1><<<dim3(NSPLIT, HKV, B), 256, 0, stream>>>(q, kc, vc, po, pml); else k_attn<0><<<dim3(NSPLIT, HKV, B), 256, 0, stream>>>(q, kc, vc, po, pml);
    k_comb<<<B * HQ, 64, 0, stream>>>(po, pml, o);
    k_o<<<dim3(8, 32), 256, 0, stream>>>(o, wo, part);
    k_res<<<16, 256, 0, stream>>>(part, xin, xout);
    xin = xout;
  }
}
```

```cpp
#include <hip/hip_runtime.h>
#include <stdint.h>

typedef unsigned u32;
#define RF(x) ((u32)__builtin_amdgcn_readfirstlane((int)(x)))
struct Params { const void* x; const void* kv[4]; const void* wq[2]; const void* wo[2]; void* out; void* ws; };
constexpr u32 WS_USED = 8413312u;
constexpr u32 LDS_BYTES = 163840;

__global__ void __launch_bounds__(256, 1) __attribute__((amdgpu_num_vgpr(90), amdgpu_num_sgpr(64))) gqa4_kernel(Params p) {
#if defined(__HIP_DEVICE_COMPILE__)
  u32 c0 = blockIdx.x, tid = threadIdx.x;
  if (0u == 1) c0 = (c0 & 7) | ((c0 >> 4) << 3) | ((c0 & 8) << 4);
  if (0u == 2) c0 = (c0 & 15) | ((c0 >> 5) << 4) | ((c0 & 16) << 3);
  if (0u == 3) c0 = (c0 & 31) | ((c0 >> 6) << 5) | ((c0 & 32) << 2);
  asm volatile(
    "s_load_dwordx2 s[72:73], %2, 0x8\n"
    "s_load_dwordx2 s[76:77], %2, 0x10\n"
    "s_load_dwordx2 s[80:81], %2, 0x18\n"
    "s_load_dwordx2 s[84:85], %2, 0x20\n"
    "v_lshrrev_b32 v90, 6, %1\n"
    "v_and_b32 v95, 63, %1\n"
    "v_readfirstlane_b32 s98, v90\n"
    "v_lshlrev_b32 v95, 4, v95\n"
    "s_lshr_b32 s88, %0, 5\n"
    "s_lshl_b32 s88, s88, 2\n"
    "s_and_b32 s99, %0, 3\n"
    "s_or_b32 s88, s88, s99\n"
    "s_lshl_b32 s88, s88, 20\n"
    "s_bfe_u32 s99, %0, 0x20003\n"
    "s_lshl_b32 s99, s99, 16\n"
    "s_or_b32 s88, s88, s99\n"
    "s_lshl_b32 s100, s98, 14\n"
    "s_or_b32 s88, s88, s100\n"
    "s_bfe_u32 s99, %0, 0x30003\n"
    "s_lshl_b32 s99, s99, 10\n"
    "s_or_b32 s66, s88, s99\n"
    "s_lshr_b32 s100, %0, 2\n"
    "s_xor_b32 s100, s100, s98\n"
    "s_and_b32 s100, s100, 1\n"
    "s_lshl_b32 s61, s100, 13\n"
    "s_or_b32 s88, s66, s61\n"
    "s_andn2_b32 s68, s88, 0x800\n"
    "s_and_b32 s99, s99, 0x800\n"
    "v_and_b32 v94, 32, v95\n"
    "v_lshlrev_b32 v94, 6, v94\n"
    "v_and_b32 v90, 0xc0, v95\n"
    "v_lshl_or_b32 v94, v90, 2, v94\n"
    "v_lshrrev_b32 v90, 3, v95\n"
    "v_and_b32 v90, 0x60, v90\n"
    "v_and_or_b32 v90, v95, 16, v90\n"
    "v_or_b32 v94, v94, v90\n"
    "v_xor_b32 v94, s99, v94\n"
    "s_brev_b32 s74, 32\n"
    "s_mov_b32 s75, 0x20000\n"
    "s_brev_b32 s78, 32\n"
    "s_mov_b32 s79, s75\n"
    "s_mov_b32 s89, 0\n"
    "s_cmp_lt_u32 %0, 128\n"
    "s_cselect_b32 s99, 5, 0\n"
    "s_getpc_b64 s[96:97]\n"
    ".Lgqa_pc:\n"
    "s_add_u32 s96, s96, .Lgqa_tr-.Lgqa_pc\n"
    "s_addc_u32 s97, s97, 0\n"
    "s_add_u32 s94, s96, 12\n"
    "s_addc_u32 s95, s97, 0\n"
    "s_lshr_b32 s100, %0, 3\n"
    "s_cmp_lg_u32 s100, 16\n"
    "s_cbranch_scc1 .L1_%=\n"
    "s_mov_b64 s[56:57], s[94:95]\n"
    "s_mov_b32 s58, .Lgqa_end-.Lgqa_top\n"
    "s_mov_b32 s59, s75\n"
    "s_lshl_b32 s100, s98, 13\n"
    "v_lshlrev_b32 v90, 3, v95\n"
    "buffer_load_dword v91, v90, s[56:59], s100 offen\n"
    ".L1_%=:\n"
    "s_waitcnt lgkmcnt(0)\n"
    ".Lgqa_tr:\n"
    "s_cmp_lt_i32 s89, s99\n"
    "s_cbranch_scc0 .Lgqa_skip\n"
    "s_setpc_b64 s[94:95]\n"
    ".Lgqa_top:\n"
    "s_xor_b32 s93, s68, 0x0\n"
    "buffer_load_dwordx4 a[0:3], v94, s[72:75], s93 offen offset:0 nt\n"
    "buffer_load_dwordx4 a[4:7], v94, s[72:75], s93 offen offset:128 nt\n"
    "s_addk_i32 s89, 1\n"
    "s_swappc_b64 s[94:95], s[96:97]\n"
    "s_xor_b32 s93, s68, 0x400\n"
    "buffer_load_dwordx4 a[16:19], v94, s[72:75], s93 offen offset:0 nt\n"
    "buffer_load_dwordx4 a[20:23], v94, s[72:75], s93 offen offset:128 nt\n"
    "s_addk_i32 s89, 1\n"
    "s_swappc_b64 s[94:95], s[96:97]\n"
    "s_xor_b32 s93, s68, 0x1000\n"
    "buffer_load_dwordx4 a[8:11], v94, s[72:75], s93 offen offset:0 nt\n"
    "buffer_load_dwordx4 a[12:15], v94, s[72:75], s93 offen offset:128 nt\n"
    "s_addk_i32 s89, 1\n"
    "s_swappc_b64 s[94:95], s[96:97]\n"
    "s_xor_b32 s93, s68, 0x1400\n"
    "buffer_load_dwordx4 a[24:27], v94, s[72:75], s93 offen offset:0 nt\n"
    "buffer_load_dwordx4 a[28:31], v94, s[72:75], s93 offen offset:128 nt\n"
    "s_addk_i32 s89, 1\n"
    "s_swappc_b64 s[94:95], s[96:97]\n"
    "s_xor_b32 s93, s68, 0x40000\n"
    "buffer_load_dwordx4 a[32:35], v94, s[72:75], s93 offen offset:0 nt\n"
    "buffer_load_dwordx4 a[36:39], v94, s[72:75], s93 offen offset:128 nt\n"
    "s_addk_i32 s89, 1\n"
    "s_swappc_b64 s[94:95], s[96:97]\n"
    "s_xor_b32 s93, s68, 0x40400\n"
    "buffer_load_dwordx4 a[48:51], v94, s[72:75], s93 offen offset:0 nt\n"
    "buffer_load_dwordx4 a[52:55], v94, s[72:75], s93 offen offset:128 nt\n"
    "s_addk_i32 s89, 1\n"
    "s_swappc_b64 s[94:95], s[96:97]\n"
    "s_xor_b32 s93, s68, 0x41000\n"
    "buffer_load_dwordx4 a[40:43], v94, s[72:75], s93 offen offset:0 nt\n"
    "buffer_load_dwordx4 a[44:47], v94, s[72:75], s93 offen offset:128 nt\n"
    "s_addk_i32 s89, 1\n"
    "s_swappc_b64 s[94:95], s[96:97]\n"
    "s_xor_b32 s93, s68, 0x41400\n"
    "buffer_load_dwordx4 a[56:59], v94, s[72:75], s93 offen offset:0 nt\n"
    "buffer_load_dwordx4 a[60:63], v94, s[72:75], s93 offen offset:128 nt\n"
    "s_addk_i32 s89, 1\n"
    "s_swappc_b64 s[94:95], s[96:97]\n"
    "s_xor_b32 s93, s68, 0x80000\n"
    "buffer_load_dwordx4 a[64:67], v94, s[72:75], s93 offen offset:0 nt\n"
    "buffer_load_dwordx4 a[68:71], v94, s[72:75], s93 offen offset:128 nt\n"
    "s_addk_i32 s89, 1\n"
    "s_swappc_b64 s[94:95], s[96:97]\n"
    "s_xor_b32 s93, s68, 0x80400\n"
    "buffer_load_dwordx4 a[80:83], v94, s[72:75], s93 offen offset:0 nt\n"
    "buffer_load_dwordx4 a[84:87], v94, s[72:75], s93 offen offset:128 nt\n"
    "s_addk_i32 s89, 1\n"
    "s_swappc_b64 s[94:95], s[96:97]\n"
    "s_xor_b32 s93, s68, 0x81000\n"
    "buffer_load_dwordx4 a[72:75], v94, s[72:75], s93 offen offset:0 nt\n"
    "buffer_load_dwordx4 a[76:79], v94, s[72:75], s93 offen offset:128 nt\n"
    "s_addk_i32 s89, 1\n"
    "s_swappc_b64 s[94:95], s[96:97]\n"
    "s_xor_b32 s93, s68, 0x81400\n"
    "buffer_load_dwordx4 a[88:91], v94, s[72:75], s93 offen offset:0 nt\n"
    "buffer_load_dwordx4 a[92:95], v94, s[72:75], s93 offen offset:128 nt\n"
    "s_addk_i32 s89, 1\n"
    "s_swappc_b64 s[94:95], s[96:97]\n"
    "s_xor_b32 s93, s68, 0xc0000\n"
    "buffer_load_dwordx4 a[96:99], v94, s[72:75], s93 offen offset:0 nt\n"
    "buffer_load_dwordx4 a[100:103], v94, s[72:75], s93 offen offset:128 nt\n"
    "s_addk_i32 s89, 1\n"
    "s_swappc_b64 s[94:95], s[96:97]\n"
    "s_xor_b32 s93, s68, 0xc0400\n"
    "buffer_load_dwordx4 a[112:115], v94, s[72:75], s93 offen offset:0 nt\n"
    "buffer_load_dwordx4 a[116:119], v94, s[72:75], s93 offen offset:128 nt\n"
    "s_addk_i32 s89, 1\n"
    "s_swappc_b64 s[94:95], s[96:97]\n"
    "s_xor_b32 s93, s68, 0xc1000\n"
    "buffer_load_dwordx4 a[104:107], v94, s[72:75], s93 offen offset:0 nt\n"
    "buffer_load_dwordx4 a[108:111], v94, s[72:75], s93 offen offset:128 nt\n"
    "s_addk_i32 s89, 1\n"
    "s_swappc_b64 s[94:95], s[96:97]\n"
    "s_xor_b32 s93, s68, 0xc1400\n"
    "buffer_load_dwordx4 a[120:123], v94, s[72:75], s93 offen offset:0 nt\n"
    "buffer_load_dwordx4 a[124:127], v94, s[72:75], s93 offen offset:128 nt\n"
    "s_addk_i32 s89, 1\n"
    "s_swappc_b64 s[94:95], s[96:97]\n"
    "s_add_u32 m0, s92, 0x0\n"
    "s_xor_b32 s93, s88, 0x0\n"
    "buffer_load_dwordx4 v95, s[76:79], s93 offen nt lds\n"
    "s_add_u32 m0, s92, 0x400\n"
    "s_xor_b32 s93, s88, 0x400\n"
    "buffer_load_dwordx4 v95, s[76:79], s93 offen nt lds\n"
    "s_addk_i32 s89, 1\n"
    "s_swappc_b64 s[94:95], s[96:97]\n"
    "s_add_u32 m0, s92, 0x800\n"
    "s_xor_b32 s93, s88, 0x800\n"
    "buffer_load_dwordx4 v95, s[76:79], s93 offen nt lds\n"
    "s_add_u32 m0, s92, 0xc00\n"
    "s_xor_b32 s93, s88, 0xc00\n"
    "buffer_load_dwordx4 v95, s[76:79], s93 offen nt lds\n"
    "s_addk_i32 s89, 1\n"
    "s_swappc_b64 s[94:95], s[96:97]\n"
    "s_add_u32 m0, s92, 0x1000\n"
    "s_xor_b32 s93, s88, 0x1000\n"
    "buffer_load_dwordx4 v95, s[76:79], s93 offen nt lds\n"
    "s_add_u32 m0, s92, 0x1400\n"
    "s_xor_b32 s93, s88, 0x1400\n"
    "buffer_load_dwordx4 v95, s[76:79], s93 offen nt lds\n"
    "s_addk_i32 s89, 1\n"
    "s_swappc_b64 s[94:95], s[96:97]\n"
    "s_add_u32 m0, s92, 0x1800\n"
    "s_xor_b32 s93, s88, 0x1800\n"
    "buffer_load_dwordx4 v95, s[76:79], s93 offen nt lds\n"
    "s_add_u32 m0, s92, 0x1c00\n"
    "s_xor_b32 s93, s88, 0x1c00\n"
    "buffer_load_dwordx4 v95, s[76:79], s93 offen nt lds\n"
    "s_addk_i32 s89, 1\n"
    "s_swappc_b64 s[94:95], s[96:97]\n"
    "s_xor_b32 s93, s88, 0x40000\n"
    "buffer_load_dwordx4 v[160:163], v95, s[76:79], s93 offen nt\n"
    "s_xor_b32 s93, s88, 0x40400\n"
    "buffer_load_dwordx4 v[164:167], v95, s[76:79], s93 offen nt\n"
    "s_addk_i32 s89, 1\n"
    "s_swappc_b64 s[94:95], s[96:97]\n"
    "s_xor_b32 s93, s88, 0x40800\n"
    "buffer_load_dwordx4 v[168:171], v95, s[76:79], s93 offen nt\n"
    "s_xor_b32 s93, s88, 0x40c00\n"
    "buffer_load_dwordx4 v[172:175], v95, s[76:79], s93 offen nt\n"
    "s_addk_i32 s89, 1\n"
    "s_swappc_b64 s[94:95], s[96:97]\n"
    "s_xor_b32 s93, s88, 0x41000\n"
    "buffer_load_dwordx4 v[176:179], v95, s[76:79], s93 offen nt\n"
    "s_xor_b32 s93, s88, 0x41400\n"
    "buffer_load_dwordx4 v[180:183], v95, s[76:79], s93 offen nt\n"
    "s_addk_i32 s89, 1\n"
    "s_swappc_b64 s[94:95], s[96:97]\n"
    "s_xor_b32 s93, s88, 0x41800\n"
    "buffer_load_dwordx4 v[184:187], v95, s[76:79], s93 offen nt\n"
    "s_xor_b32 s93, s88, 0x41c00\n"
    "buffer_load_dwordx4 v[188:191], v95, s[76:79], s93 offen nt\n"
    "s_addk_i32 s89, 1\n"
    "s_swappc_b64 s[94:95], s[96:97]\n"
    "v_mov_b32 v77, 1\n"
    "s_mov_b64 exec, 1\n"
    "buffer_atomic_add v77, off, s[0:3], s67 sc0\n"
    "s_mov_b64 exec, -1\n"
    "s_add_u32 s67, s67, 0x34000\n"
    "s_xor_b32 s93, s88, 0x80000\n"
    "buffer_load_dwordx4 v[192:195], v95, s[76:79], s93 offen nt\n"
    "s_xor_b32 s93, s88, 0x80400\n"
    "buffer_load_dwordx4 v[196:199], v95, s[76:79], s93 offen nt\n"
    "s_addk_i32 s89, 1\n"
    "s_swappc_b64 s[94:95], s[96:97]\n"
    "s_xor_b32 s93, s88, 0x80800\n"
    "buffer_load_dwordx4 v[200:203], v95, s[76:79], s93 offen nt\n"
    "s_xor_b32 s93, s88, 0x80c00\n"
    "buffer_load_dwordx4 v[204:207], v95, s[76:79], s93 offen nt\n"
    "s_addk_i32 s89, 1\n"
    "s_swappc_b64 s[94:95], s[96:97]\n"
    "s_xor_b32 s93, s88, 0x81000\n"
    "buffer_load_dwordx4 v[208:211], v95, s[76:79], s93 offen nt\n"
    "s_xor_b32 s93, s88, 0x81400\n"
    "buffer_load_dwordx4 v[212:215], v95, s[76:79], s93 offen nt\n"
    "s_addk_i32 s89, 1\n"
    "s_swappc_b64 s[94:95], s[96:97]\n"
    "s_xor_b32 s93, s88, 0x81800\n"
    "buffer_load_dwordx4 v[216:219], v95, s[76:79], s93 offen nt\n"
    "s_xor_b32 s93, s88, 0x81c00\n"
    "buffer_load_dwordx4 v[220:223], v95, s[76:79], s93 offen nt\n"
    "s_addk_i32 s89, 1\n"
    "s_swappc_b64 s[94:95], s[96:97]\n"
    "s_xor_b32 s93, s88, 0xc0000\n"
    "buffer_load_dwordx4 v[224:227], v95, s[76:79], s93 offen nt\n"
    "s_xor_b32 s93, s88, 0xc0400\n"
    "buffer_load_dwordx4 v[228:231], v95, s[76:79], s93 offen nt\n"
    "s_addk_i32 s89, 1\n"
    "s_swappc_b64 s[94:95], s[96:97]\n"
    "s_xor_b32 s93, s88, 0xc0800\n"
    "buffer_load_dwordx4 v[232:235], v95, s[76:79], s93 offen nt\n"
    "s_xor_b32 s93, s88, 0xc0c00\n"
    "buffer_load_dwordx4 v[236:239], v95, s[76:79], s93 offen nt\n"
    "s_addk_i32 s89, 1\n"
    "s_swappc_b64 s[94:95], s[96:97]\n"
    "s_xor_b32 s93, s88, 0xc1000\n"
    "buffer_load_dwordx4 v[240:243], v95, s[76:79], s93 offen nt\n"
    "s_xor_b32 s93, s88, 0xc1400\n"
    "buffer_load_dwordx4 v[244:247], v95, s[76:79], s93 offen nt\n"
    "s_addk_i32 s89, 1\n"
    "s_swappc_b64 s[94:95], s[96:97]\n"
    "s_xor_b32 s93, s88, 0xc1800\n"
    "buffer_load_dwordx4 v[248:251], v95, s[76:79], s93 offen nt\n"
    "s_xor_b32 s93, s88, 0xc1c00\n"
    "buffer_load_dwordx4 v[252:255], v95, s[76:79], s93 offen nt\n"
    "s_addk_i32 s89, 1\n"
    "s_swappc_b64 s[94:95], s[96:97]\n"
    "s_xor_b32 s93, s68, 0x0\n"
    "buffer_load_dwordx4 a[128:131], v94, s[80:83], s93 offen offset:0 nt\n"
    "buffer_load_dwordx4 a[132:135], v94, s[80:83], s93 offen offset:128 nt\n"
    "s_addk_i32 s89, 1\n"
    "s_swappc_b64 s[94:95], s[96:97]\n"
    "s_xor_b32 s93, s68, 0x400\n"
    "buffer_load_dwordx4 a[144:147], v94, s[80:83], s93 offen offset:0 nt\n"
    "buffer_load_dwordx4 a[148:151], v94, s[80:83], s93 offen offset:128 nt\n"
    "s_addk_i32 s89, 1\n"
    "s_swappc_b64 s[94:95], s[96:97]\n"
    "s_xor_b32 s93, s68, 0x1000\n"
    "buffer_load_dwordx4 a[136:139], v94, s[80:83], s93 offen offset:0 nt\n"
    "buffer_load_dwordx4 a[140:143], v94, s[80:83], s93 offen offset:128 nt\n"
    "s_addk_i32 s89, 1\n"
    "s_swappc_b64 s[94:95], s[96:97]\n"
    "s_xor_b32 s93, s68, 0x1400\n"
    "buffer_load_dwordx4 a[152:155], v94, s[80:83], s93 offen offset:0 nt\n"
    "buffer_load_dwordx4 a[156:159], v94, s[80:83], s93 offen offset:128 nt\n"
    "s_addk_i32 s89, 1\n"
    "s_swappc_b64 s[94:95], s[96:97]\n"
    "s_xor_b32 s93, s68, 0x40000\n"
    "buffer_load_dwordx4 a[160:163], v94, s[80:83], s93 offen offset:0 nt\n"
    "buffer_load_dwordx4 a[164:167], v94, s[80:83], s93 offen offset:128 nt\n"
    "s_addk_i32 s89, 1\n"
    "s_swappc_b64 s[94:95], s[96:97]\n"
    "s_xor_b32 s93, s68, 0x40400\n"
    "buffer_load_dwordx4 a[176:179], v94, s[80:83], s93 offen offset:0 nt\n"
    "buffer_load_dwordx4 a[180:183], v94, s[80:83], s93 offen offset:128 nt\n"
    "s_addk_i32 s89, 1\n"
    "s_swappc_b64 s[94:95], s[96:97]\n"
    "s_xor_b32 s93, s68, 0x41000\n"
    "buffer_load_dwordx4 a[168:171], v94, s[80:83], s93 offen offset:0 nt\n"
    "buffer_load_dwordx4 a[172:175], v94, s[80:83], s93 offen offset:128 nt\n"
    "s_addk_i32 s89, 1\n"
    "s_swappc_b64 s[94:95], s[96:97]\n"
    "s_xor_b32 s93, s68, 0x41400\n"
    "buffer_load_dwordx4 a[184:187], v94, s[80:83], s93 offen offset:0 nt\n"
    "buffer_load_dwordx4 a[188:191], v94, s[80:83], s93 offen offset:128 nt\n"
    "s_addk_i32 s89, 1\n"
    "s_swappc_b64 s[94:95], s[96:97]\n"
    "s_xor_b32 s93, s68, 0x80000\n"
    "buffer_load_dwordx4 a[192:195], v94, s[80:83], s93 offen offset:0 nt\n"
    "buffer_load_dwordx4 a[196:199], v94, s[80:83], s93 offen offset:128 nt\n"
    "s_addk_i32 s89, 1\n"
    "s_swappc_b64 s[94:95], s[96:97]\n"
    "s_xor_b32 s93, s68, 0x80400\n"
    "buffer_load_dwordx4 a[208:211], v94, s[80:83], s93 offen offset:0 nt\n"
    "buffer_load_dwordx4 a[212:215], v94, s[80:83], s93 offen offset:128 nt\n"
    "s_addk_i32 s89, 1\n"
    "s_swappc_b64 s[94:95], s[96:97]\n"
    "s_xor_b32 s93, s68, 0x81000\n"
    "buffer_load_dwordx4 a[200:203], v94, s[80:83], s93 offen offset:0 nt\n"
    "buffer_load_dwordx4 a[204:207], v94, s[80:83], s93 offen offset:128 nt\n"
    "s_addk_i32 s89, 1\n"
    "s_swappc_b64 s[94:95], s[96:97]\n"
    "s_xor_b32 s93, s68, 0x81400\n"
    "buffer_load_dwordx4 a[216:219], v94, s[80:83], s93 offen offset:0 nt\n"
    "buffer_load_dwordx4 a[220:223], v94, s[80:83], s93 offen offset:128 nt\n"
    "s_addk_i32 s89, 1\n"
    "s_swappc_b64 s[94:95], s[96:97]\n"
    "s_xor_b32 s93, s68, 0xc0000\n"
    "buffer_load_dwordx4 a[224:227], v94, s[80:83], s93 offen offset:0 nt\n"
    "buffer_load_dwordx4 a[228:231], v94, s[80:83], s93 offen offset:128 nt\n"
    "s_addk_i32 s89, 1\n"
    "s_swappc_b64 s[94:95], s[96:97]\n"
    "s_xor_b32 s93, s68, 0xc0400\n"
    "buffer_load_dwordx4 a[240:243], v94, s[80:83], s93 offen offset:0 nt\n"
    "buffer_load_dwordx4 a[244:247], v94, s[80:83], s93 offen offset:128 nt\n"
    "s_addk_i32 s89, 1\n"
    "s_swappc_b64 s[94:95], s[96:97]\n"
    "s_xor_b32 s93, s68, 0xc1000\n"
    "buffer_load_dwordx4 a[232:235], v94, s[80:83], s93 offen offset:0 nt\n"
    "buffer_load_dwordx4 a[236:239], v94, s[80:83], s93 offen offset:128 nt\n"
    "s_addk_i32 s89, 1\n"
    "s_swappc_b64 s[94:95], s[96:97]\n"
    "s_xor_b32 s93, s68, 0xc1400\n"
    "buffer_load_dwordx4 a[248:251], v94, s[80:83], s93 offen offset:0 nt\n"
    "buffer_load_dwordx4 a[252:255], v94, s[80:83], s93 offen offset:128 nt\n"
    "s_addk_i32 s89, 1\n"
    "s_swappc_b64 s[94:95], s[96:97]\n"
    "s_add_u32 m0, s92, 0x2000\n"
    "s_xor_b32 s93, s88, 0x0\n"
    "buffer_load_dwordx4 v95, s[84:87], s93 offen nt lds\n"
    "s_add_u32 m0, s92, 0x2400\n"
    "s_xor_b32 s93, s88, 0x400\n"
    "buffer_load_dwordx4 v95, s[84:87], s93 offen nt lds\n"
    "s_addk_i32 s89, 1\n"
    "s_swappc_b64 s[94:95], s[96:97]\n"
    "s_add_u32 m0, s92, 0x2800\n"
    "s_xor_b32 s93, s88, 0x800\n"
    "buffer_load_dwordx4 v95, s[84:87], s93 offen nt lds\n"
    "s_add_u32 m0, s92, 0x2c00\n"
    "s_xor_b32 s93, s88, 0xc00\n"
    "buffer_load_dwordx4 v95, s[84:87], s93 offen nt lds\n"
    "s_addk_i32 s89, 1\n"
    "s_swappc_b64 s[94:95], s[96:97]\n"
    "s_add_u32 m0, s92, 0x3000\n"
    "s_xor_b32 s93, s88, 0x1000\n"
    "buffer_load_dwordx4 v95, s[84:87], s93 offen nt lds\n"
    "s_add_u32 m0, s92, 0x3400\n"
    "s_xor_b32 s93, s88, 0x1400\n"
    "buffer_load_dwordx4 v95, s[84:87], s93 offen nt lds\n"
    "s_addk_i32 s89, 1\n"
    "s_swappc_b64 s[94:95], s[96:97]\n"
    "s_add_u32 m0, s92, 0x3800\n"
    "s_xor_b32 s93, s88, 0x1800\n"
    "buffer_load_dwordx4 v95, s[84:87], s93 offen nt lds\n"
    "s_add_u32 m0, s92, 0x3c00\n"
    "s_xor_b32 s93, s88, 0x1c00\n"
    "buffer_load_dwordx4 v95, s[84:87], s93 offen nt lds\n"
    "s_addk_i32 s89, 1\n"
    "s_swappc_b64 s[94:95], s[96:97]\n"
    "s_add_u32 m0, s92, 0x4000\n"
    "s_xor_b32 s93, s88, 0x40000\n"
    "buffer_load_dwordx4 v95, s[84:87], s93 offen nt lds\n"
    "s_add_u32 m0, s92, 0x4400\n"
    "s_xor_b32 s93, s88, 0x40400\n"
    "buffer_load_dwordx4 v95, s[84:87], s93 offen nt lds\n"
    "s_addk_i32 s89, 1\n"
    "s_swappc_b64 s[94:95], s[96:97]\n"
    "s_add_u32 m0, s92, 0x4800\n"
    "s_xor_b32 s93, s88, 0x40800\n"
    "buffer_load_dwordx4 v95, s[84:87], s93 offen nt lds\n"
    "s_add_u32 m0, s92, 0x4c00\n"
    "s_xor_b32 s93, s88, 0x40c00\n"
    "buffer_load_dwordx4 v95, s[84:87], s93 offen nt lds\n"
    "s_addk_i32 s89, 1\n"
    "s_swappc_b64 s[94:95], s[96:97]\n"
    "s_add_u32 m0, s92, 0x5000\n"
    "s_xor_b32 s93, s88, 0x41000\n"
    "buffer_load_dwordx4 v95, s[84:87], s93 offen nt lds\n"
    "s_add_u32 m0, s92, 0x5400\n"
    "s_xor_b32 s93, s88, 0x41400\n"
    "buffer_load_dwordx4 v95, s[84:87], s93 offen nt lds\n"
    "s_addk_i32 s89, 1\n"
    "s_swappc_b64 s[94:95], s[96:97]\n"
    "s_add_u32 m0, s92, 0x5800\n"
    "s_xor_b32 s93, s88, 0x41800\n"
    "buffer_load_dwordx4 v95, s[84:87], s93 offen nt lds\n"
    "s_add_u32 m0, s92, 0x5c00\n"
    "s_xor_b32 s93, s88, 0x41c00\n"
    "buffer_load_dwordx4 v95, s[84:87], s93 offen nt lds\n"
    "s_addk_i32 s89, 1\n"
    "s_swappc_b64 s[94:95], s[96:97]\n"
    "v_mov_b32 v77, 1\n"
    "s_mov_b64 exec, 1\n"
    "buffer_atomic_add v77, off, s[0:3], s67 sc0\n"
    "s_mov_b64 exec, -1\n"
    "s_add_u32 s67, s67, 0x9c000\n"
    "s_xor_b32 s93, s88, 0x80000\n"
    "buffer_load_dwordx4 v[96:99], v95, s[84:87], s93 offen nt\n"
    "s_xor_b32 s93, s88, 0x80400\n"
    "buffer_load_dwordx4 v[100:103], v95, s[84:87], s93 offen nt\n"
    "s_addk_i32 s89, 1\n"
    "s_swappc_b64 s[94:95], s[96:97]\n"
    "s_xor_b32 s93, s88, 0x80800\n"
    "buffer_load_dwordx4 v[104:107], v95, s[84:87], s93 offen nt\n"
    "s_xor_b32 s93, s88, 0x80c00\n"
    "buffer_load_dwordx4 v[108:111], v95, s[84:87], s93 offen nt\n"
    "s_addk_i32 s89, 1\n"
    "s_swappc_b64 s[94:95], s[96:97]\n"
    "s_xor_b32 s93, s88, 0x81000\n"
    "buffer_load_dwordx4 v[112:115], v95, s[84:87], s93 offen nt\n"
    "s_xor_b32 s93, s88, 0x81400\n"
    "buffer_load_dwordx4 v[116:119], v95, s[84:87], s93 offen nt\n"
    "s_addk_i32 s89, 1\n"
    "s_swappc_b64 s[94:95], s[96:97]\n"
    "s_xor_b32 s93, s88, 0x81800\n"
    "buffer_load_dwordx4 v[120:123], v95, s[84:87], s93 offen nt\n"
    "s_xor_b32 s93, s88, 0x81c00\n"
    "buffer_load_dwordx4 v[124:127], v95, s[84:87], s93 offen nt\n"
    "s_addk_i32 s89, 1\n"
    "s_swappc_b64 s[94:95], s[96:97]\n"
    "s_xor_b32 s93, s88, 0xc0000\n"
    "buffer_load_dwordx4 v[128:131], v95, s[84:87], s93 offen nt\n"
    "s_xor_b32 s93, s88, 0xc0400\n"
    "buffer_load_dwordx4 v[132:135], v95, s[84:87], s93 offen nt\n"
    "s_addk_i32 s89, 1\n"
    "s_swappc_b64 s[94:95], s[96:97]\n"
    "s_xor_b32 s93, s88, 0xc0800\n"
    "buffer_load_dwordx4 v[136:139], v95, s[84:87], s93 offen nt\n"
    "s_xor_b32 s93, s88, 0xc0c00\n"
    "buffer_load_dwordx4 v[140:143], v95, s[84:87], s93 offen nt\n"
    "s_addk_i32 s89, 1\n"
    "s_swappc_b64 s[94:95], s[96:97]\n"
    "s_xor_b32 s93, s88, 0xc1000\n"
    "buffer_load_dwordx4 v[144:147], v95, s[84:87], s93 offen nt\n"
    "s_xor_b32 s93, s88, 0xc1400\n"
    "buffer_load_dwordx4 v[148:151], v95, s[84:87], s93 offen nt\n"
    "s_addk_i32 s89, 1\n"
    "s_swappc_b64 s[94:95], s[96:97]\n"
    "s_xor_b32 s93, s88, 0xc1800\n"
    "buffer_load_dwordx4 v[152:155], v95, s[84:87], s93 offen nt\n"
    "s_xor_b32 s93, s88, 0xc1c00\n"
    "buffer_load_dwordx4 v[156:159], v95, s[84:87], s93 offen nt\n"
    "s_addk_i32 s89, 1\n"
    "s_swappc_b64 s[94:95], s[96:97]\n"
    "s_branch .Lgqa_top\n"
    ".Lgqa_skip:\n"
    "s_mul_i32 s92, s98, 0x6000\n"
    "s_add_u32 s92, s92, 0x10000\n"
    "s_brev_b32 s78, 32\n"
    "s_mov_b32 s79, 0x20000\n"
    "s_brev_b32 s82, 32\n"
    "s_mov_b32 s83, 0x20000\n"
    "s_brev_b32 s86, 32\n"
    "s_mov_b32 s87, 0x20000\n"
    "s_mov_b32 s91, 0\n"
    "s_cmp_lt_u32 %0, 128\n"
    "s_cselect_b32 s90, 24, 0\n"
    "s_memtime s[58:59]\n"
    :: "s"(c0), "v"(tid), "s"(__builtin_amdgcn_kernarg_segment_ptr()) : "v255", "a255", "s101", "scc", "memory");
  asm volatile("" : "+v"(tid));
  asm volatile("" : "+s"(c0));
  const u32 lane = tid & 63, w = __builtin_amdgcn_readfirstlane(tid >> 6), c = c0;
  const u32 kvh = c & 7, jj = (c >> 3) & 15, bq = jj >> 3;
  const u32 kva = 1u ? (c & 3) + 4 * ((jj >> 2) & 1) : kvh, row = 1u ? 8 * bq + (jj & 3) + 4 * (kvh >> 2) : jj;
  const u32 g4 = lane >> 4, m16 = lane & 15;
  const u32 vQ = (4 * (lane & 3) + (g4 >> 1)) * 128 + (2 * (g4 & 1) + ((lane >> 2) & 1)) * 16;
  const u32 vMW = 0u + w * 2048 + ((lane & 3) * 4 + g4) * 128 + ((m16 >> 2) & 1) * 16;
  const u32 vMLW = 8192u + (lane & 3) * 32 + w * 8, vMR = 0u + w * 512 + lane * 8;
  const u32 vPA = g4 * 1024 + m16 * 4, vWo = 16384u + w * 8192 + (8 * g4 + (m16 >> 2)) * 64 + (g4 & 1) * 32 + (lane & 3) * 8;
  const u32 vA = m16 * 64 + g4 * 16, vL = ((g4 >> 1) * 8 + (lane & 3) + 4 * (((lane >> 2) ^ g4) & 1)) * 64;
  const u32 vPB = (((lane & 31) >> 3) * 2 + (lane >> 5)) * 16 + (lane & 7) * 2;
  const u32 vWq = 16384u + w * 8192 + (64 * (g4 >> 1) + 4 * (g4 & 1) + (m16 >> 2)) * 64 + (g4 & 1) * 32 + (lane & 3) * 8;
  const u32 dWo = ((8 * ((lane >> 2) & 3) + 2 * (lane >> 5) + (g4 & 1)) << 8) | (((lane & 3) ^ (2 * (lane >> 5))) << 4);
  const u32 dWq = (lane & 3) ^ (2 * (g4 & 1));
  const u32 vRQW = 10368u + (g4 & 1) * 512 + m16 * 16 + w * 4;
  const u32 vX = (lane & 1) * 1024 + w * 256 + (g4 >> 1) * 128 + (m16 >> 1) * 16 + (g4 & 1) * 8;
  const u32 vR2W = 8320u + (((g4 >> 1) * 32 + m16) * 8 + w * 2 + (g4 & 1)) * 4;
  const u32 vRQR = (g4 & 1) * 1024 + (g4 >> 1) * 256 + m16 * 16;
  asm volatile(
    "s_mov_b32 s2, 0x806080\n"
    "s_mov_b32 s3, 0x20000\n"
    "s_mov_b32 s7, 0x20000\n"
    "s_mov_b32 s52, 0\n"
    "s_mov_b32 s53, 0\n"
    "s_mov_b32 s38, 0x05040100\n"
    "s_mov_b32 s39, 0x07060302\n"
    "s_mov_b32 s40, 0x3e0293ee\n"
    "s_mov_b32 s41, 0x40000\n"
    "s_mov_b32 s48, 0x00ff00ff\n"
    "s_mov_b32 s49, 0x00ff00ff\n"
    "s_mov_b32 s50, 0x55555555\n"
    "s_mov_b32 s51, 0x55555555\n"
    "s_lshl_b32 s54, s21, 13\n"
    "s_add_u32 s54, s54, 0x4000\n"
    "s_lshr_b32 s44, s20, 7\n"
    "s_mul_i32 s43, s44, 0x400000\n"
    "s_add_u32 s67, s43, s25\n"
    "s_cmp_ge_u32 s20, 128\n"
    "s_cbranch_scc1 .L2_%=\n"
    "s_mov_b64 s[4:5], s[8:9]\n"
    "s_mov_b32 s6, 0x2000\n"
    "v_lshrrev_b32 v0, 16, v86\n"
    "s_movk_i32 s44, 0x1000\n"
    "buffer_load_dwordx2 v[88:89], v0, s[4:7], 0 offen\n"
    "buffer_load_dwordx2 v[90:91], v0, s[4:7], s44 offen\n"
    "s_mov_b32 s44, 0x804000\n"
    "s_mov_b64 exec, 1\n"
    "buffer_load_dwordx2 v[8:9], off, s[0:3], s44 sc1\n"
    "s_mov_b64 exec, -1\n"
    "s_memrealtime s[100:101]\n"
    "s_waitcnt lgkmcnt(0)\n"
    "s_add_u32 s65, s100, 260\n"
    "s_lshl_b32 s63, s20, 11\n"
    "s_and_b32 s63, s63, 0x2000\n"
    "s_cmp_lt_i32 s91, 7\n"
    "s_cbranch_scc1 .L4_%=\n"
    ".L5_%=:\n"
    "s_cmp_lt_i32 s91, 7\n"
    "s_cbranch_scc1 .L7_%=\n"
    ".L8_%=:\n"
    "v_readfirstlane_b32 s69, v8\n"
    "v_readfirstlane_b32 s37, v9\n"
    "s_add_u32 s69, s69, 1\n"
    "s_add_u32 s99, s58, 9000\n"
    ".L10_%=:\n"
    "s_memtime s[100:101]\n"
    "s_waitcnt lgkmcnt(0)\n"
    "s_sub_u32 s98, s100, s99\n"
    "s_cmp_lt_i32 s98, 0\n"
    "s_cbranch_scc0 .L11_%=\n"
    "s_cmp_lt_i32 s89, s90\n"
    "s_cbranch_scc0 .L10_%=\n"
    "s_waitcnt vmcnt(8)\n"
    "s_sub_i32 s98, s89, 4\n"
    "s_max_i32 s91, s91, s98\n"
    "s_swappc_b64 s[96:97], s[94:95]\n"
    "s_branch .L10_%=\n"
    ".L11_%=:\n"
    "s_mov_b32 s44, 0x800000\n"
    "s_and_b32 s45, s20, 3\n"
    "s_lshl_b32 s45, s45, 12\n"
    "v_lshlrev_b32 v38, 3, v95\n"
    "v_add_u32 v38, s45, v38\n"
    "v_writelane_b32 v38, 0, 32\n"
    "v_lshrrev_b32 v39, 1, v95\n"
    "s_mov_b32 s45, 0x804080\n"
    ".L16_%=:\n"
    "s_mov_b32 exec_hi, 1\n"
    "buffer_load_dwordx2 v[36:37], v38, s[0:3], s44 offen sc1\n"
    "s_mov_b64 exec, -1\n"
    "buffer_load_dwordx2 v[42:43], v39, s[0:3], s45 offen sc1\n"
    "buffer_load_dwordx2 v[44:45], v39, s[0:3], s45 offen offset:512 sc1\n"
    "s_cmp_lt_i32 s89, s90\n"
    "s_cbranch_scc1 .L12_%=\n"
    "s_waitcnt vmcnt(0)\n"
    "s_mov_b32 s91, s89\n"
    ".L13_%=:\n"
    "s_mov_b32 s98, 0\n"
    "v_xor_b32 v36, s69, v36\n"
    "v_xor_b32 v37, s37, v37\n"
    "v_or_b32 v36, v36, v37\n"
    "v_cmp_ne_u32 vcc, 0, v36\n"
    "s_and_b32 s98, vcc_hi, 1\n"
    "s_or_b32 s98, s98, vcc_lo\n"
    "v_xor_b32 v42, s69, v42\n"
    "v_xor_b32 v44, s69, v44\n"
    "v_or_b32 v42, v42, v44\n"
    "v_cmp_ne_u32 vcc, 0, v42\n"
    "s_or_b32 s98, s98, vcc_lo\n"
    "s_or_b32 s98, s98, vcc_hi\n"
    "s_cbranch_scc0 .L17_%=\n"
    "s_addk_i32 s41, -1\n"
    "s_cmp_gt_i32 s41, 0\n"
    "s_cbranch_scc0 .L17_%=\n"
    "s_sleep 8\n"
    "s_branch .L16_%=\n"
    ".L17_%=:\n"
    "v_and_b32 v18, 0xffff, v81\n"
    "s_mov_b32 s45, s22\n"
    "buffer_load_dwordx4 v[0:3], v18, s[0:3], s45 offen sc1\n"
    "buffer_load_dwordx4 v[4:7], v18, s[0:3], s45 offen offset:256 sc1\n"
    "s_addk_i32 s90, 8\n"
    "s_swappc_b64 s[96:97], s[94:95]\n"
    "s_swappc_b64 s[96:97], s[94:95]\n"
    "s_swappc_b64 s[96:97], s[94:95]\n"
    "s_swappc_b64 s[96:97], s[94:95]\n"
    "v_readfirstlane_b32 s44, v43\n"
    "v_and_b32 v42, 0xfff, v43\n"
    "v_bfe_u32 v43, v43, 12, 12\n"
    "v_and_b32 v44, 0xfff, v45\n"
    "v_bfe_u32 v45, v45, 12, 12\n"
    "s_nop 0\n"
    "v_min_u32_dpp v44, v44, v44 quad_perm:[1,0,3,2] row_mask:0xf bank_mask:0xf\n"
    "v_min_u32_dpp v45, v45, v45 quad_perm:[1,0,3,2] row_mask:0xf bank_mask:0xf\n"
    "s_nop 0\n"
    "v_min_u32_dpp v44, v44, v44 quad_perm:[2,3,0,1] row_mask:0xf bank_mask:0xf\n"
    "v_min_u32_dpp v45, v45, v45 quad_perm:[2,3,0,1] row_mask:0xf bank_mask:0xf\n"
    "v_min_u32_dpp v42, v42, v42 row_ror:4 row_mask:0xf bank_mask:0xf\n"
    "v_min_u32_dpp v43, v43, v43 row_ror:4 row_mask:0xf bank_mask:0xf\n"
    "v_min_u32_dpp v44, v44, v44 row_ror:4 row_mask:0xf bank_mask:0xf\n"
    "v_min_u32_dpp v45, v45, v45 row_ror:4 row_mask:0xf bank_mask:0xf\n"
    "v_min_u32_dpp v42, v42, v42 row_ror:8 row_mask:0xf bank_mask:0xf\n"
    "v_min_u32_dpp v43, v43, v43 row_ror:8 row_mask:0xf bank_mask:0xf\n"
    "v_min_u32_dpp v44, v44, v44 row_ror:8 row_mask:0xf bank_mask:0xf\n"
    "v_min_u32_dpp v45, v45, v45 row_ror:8 row_mask:0xf bank_mask:0xf\n"
    "v_mov_b32 v46, v42\n"
    "v_mov_b32 v47, v43\n"
    "v_mov_b32 v48, v44\n"
    "v_mov_b32 v49, v45\n"
    "v_permlane16_swap_b32 v42, v46\n"
    "v_permlane16_swap_b32 v43, v47\n"
    "v_permlane16_swap_b32 v44, v48\n"
    "v_permlane16_swap_b32 v45, v49\n"
    "v_min_u32 v42, v42, v46\n"
    "v_min_u32 v43, v43, v47\n"
    "v_min_u32 v44, v44, v48\n"
    "v_min_u32 v45, v45, v49\n"
    "v_mov_b32 v46, v42\n"
    "v_mov_b32 v47, v43\n"
    "v_mov_b32 v48, v44\n"
    "v_mov_b32 v49, v45\n"
    "v_permlane32_swap_b32 v42, v46\n"
    "v_permlane32_swap_b32 v43, v47\n"
    "v_permlane32_swap_b32 v44, v48\n"
    "v_permlane32_swap_b32 v45, v49\n"
    "v_min_u32 v42, v42, v46\n"
    "v_min_u32 v43, v43, v47\n"
    "v_min_u32 v44, v44, v48\n"
    "v_min_u32 v45, v45, v49\n"
    "v_cmp_gt_u32 vcc, v42, v43\n"
    "s_mov_b32 s46, vcc_lo\n"
    "v_cmp_gt_u32 vcc, v44, v45\n"
    "s_mov_b32 s71, vcc_lo\n"
    "s_lshr_b32 s44, s44, 24\n"
    "s_and_b32 s45, s20, 7\n"
    "s_add_u32 s44, s45, s44\n"
    "s_lshr_b32 s44, s44, 2\n"
    "s_lshr_b32 s45, s20, 3\n"
    "s_add_u32 s98, s72, s66\n"
    "s_xor_b32 s98, s98, s72\n"
    "s_lshr_b32 s98, s98, 21\n"
    "s_lshr_b32 s100, s46, 0\n"
    "s_xor_b32 s98, s98, s100\n"
    "s_add_u32 s99, s76, s66\n"
    "s_xor_b32 s99, s99, s76\n"
    "s_lshr_b32 s99, s99, 21\n"
    "s_lshr_b32 s100, s46, 1\n"
    "s_xor_b32 s99, s99, s100\n"
    "s_xor_b32 s99, s99, s98\n"
    "s_and_b32 s99, s99, s45\n"
    "s_xor_b32 s98, s98, s99\n"
    "s_xor_b32 s98, s98, s44\n"
    "s_and_b32 s98, s98, 1\n"
    "s_lshl_b32 s61, s98, 13\n"
    "s_add_u32 s98, s80, s66\n"
    "s_xor_b32 s98, s98, s80\n"
    "s_lshr_b32 s98, s98, 21\n"
    "s_lshr_b32 s100, s46, 2\n"
    "s_xor_b32 s98, s98, s100\n"
    "s_add_u32 s99, s84, s66\n"
    "s_xor_b32 s99, s99, s84\n"
    "s_lshr_b32 s99, s99, 21\n"
    "s_lshr_b32 s100, s46, 3\n"
    "s_xor_b32 s99, s99, s100\n"
    "s_xor_b32 s99, s99, s98\n"
    "s_and_b32 s99, s99, s45\n"
    "s_xor_b32 s98, s98, s99\n"
    "s_xor_b32 s98, s98, s44\n"
    "s_and_b32 s98, s98, 1\n"
    "s_lshl_b32 s62, s98, 13\n"
    "s_xor_b32 s71, s71, s44\n"
    "s_lshr_b32 s98, s20, 2\n"
    "s_lshr_b32 s99, s20, 5\n"
    "s_xor_b32 s98, s98, s99\n"
    "s_xor_b32 s44, s71, s98\n"
    "s_xor_b32 s45, s71, 1\n"
    "s_lshr_b32 s98, s22, 13\n"
    "s_xor_b32 s98, s98, s71\n"
    "s_and_b32 s98, s98, 1\n"
    "s_lshl_b32 s98, s98, 21\n"
    "s_or_b32 s22, s22, s98\n"
    "s_lshr_b32 s98, s26, 13\n"
    "s_xor_b32 s98, s98, s71\n"
    "s_and_b32 s98, s98, 1\n"
    "s_lshl_b32 s98, s98, 21\n"
    "s_or_b32 s26, s26, s98\n"
    "s_lshr_b32 s98, s27, 13\n"
    "s_xor_b32 s98, s98, s71\n"
    "s_and_b32 s98, s98, 1\n"
    "s_lshl_b32 s98, s98, 21\n"
    "s_or_b32 s27, s27, s98\n"
    "s_lshr_b32 s98, s29, 13\n"
    "s_xor_b32 s98, s98, s71\n"
    "s_and_b32 s98, s98, 1\n"
    "s_lshl_b32 s98, s98, 21\n"
    "s_or_b32 s29, s29, s98\n"
    "s_lshr_b32 s98, s23, 13\n"
    "s_xor_b32 s98, s98, s44\n"
    "s_and_b32 s98, s98, 1\n"
    "s_lshl_b32 s98, s98, 21\n"
    "s_or_b32 s23, s23, s98\n"
    "s_lshr_b32 s98, s24, 13\n"
    "s_xor_b32 s98, s98, s44\n"
    "s_and_b32 s98, s98, 1\n"
    "s_lshl_b32 s98, s98, 21\n"
    "s_or_b32 s24, s24, s98\n"
    "s_lshr_b32 s98, s28, 13\n"
    "s_xor_b32 s98, s98, s45\n"
    "s_and_b32 s98, s98, 1\n"
    "s_lshl_b32 s98, s98, 21\n"
    "s_or_b32 s28, s28, s98\n"
    "s_lshr_b32 s98, s30, 13\n"
    "s_xor_b32 s98, s98, s45\n"
    "s_and_b32 s98, s98, 1\n"
    "s_lshl_b32 s98, s98, 21\n"
    "s_or_b32 s30, s30, s98\n"
    "s_waitcnt vmcnt(8)\n"
    ".L3_%=:\n"
    "s_add_u32 s34, s43, 0x0\n"
    "s_mov_b32 s55, 0\n"
    "s_lshr_b32 s42, s63, 5\n"
    "s_mov_b32 s35, 0\n"
    "s_movk_i32 s36, 32\n"
    "s_mov_b32 s47, 0\n"
    "s_mov_b32 s64, -1\n"
    "s_movk_i32 s70, 0\n"
    "s_cmp_lt_u32 s20, 128\n"
    "s_cselect_b32 s55, 2, 0\n"
    "s_call_b64 s[56:57], .Latt1_%=\n"
    "s_cmp_ge_u32 s20, 128\n"
    "s_cbranch_scc1 .Lhw_%=\n"
    "s_mov_b64 s[4:5], s[16:17]\n"
    "s_mov_b32 s6, 0x400000\n"
    "v_lshrrev_b32 v0, 8, v80\n"
    "v_lshlrev_b32 v0, 10, v0\n"
    "v_and_or_b32 v0, v80, 48, v0\n"
    "s_add_u32 m0, s54, 0x0\n"
    "s_add_u32 s71, s32, 0x0\n"
    "buffer_load_dwordx4 v0, s[4:7], s71 offen lds\n"
    "s_add_u32 m0, s54, 0x400\n"
    "s_add_u32 s71, s32, 0x1000\n"
    "buffer_load_dwordx4 v0, s[4:7], s71 offen lds\n"
    "s_add_u32 m0, s54, 0x800\n"
    "s_add_u32 s71, s32, 0x8000\n"
    "buffer_load_dwordx4 v0, s[4:7], s71 offen lds\n"
    "s_add_u32 m0, s54, 0xc00\n"
    "s_add_u32 s71, s32, 0x9000\n"
    "buffer_load_dwordx4 v0, s[4:7], s71 offen lds\n"
    "s_add_u32 m0, s54, 0x1000\n"
    "s_add_u32 s71, s32, 0x10000\n"
    "buffer_load_dwordx4 v0, s[4:7], s71 offen lds\n"
    "s_add_u32 m0, s54, 0x1400\n"
    "s_add_u32 s71, s32, 0x11000\n"
    "buffer_load_dwordx4 v0, s[4:7], s71 offen lds\n"
    "s_add_u32 m0, s54, 0x1800\n"
    "s_add_u32 s71, s32, 0x18000\n"
    "buffer_load_dwordx4 v0, s[4:7], s71 offen lds\n"
    "s_add_u32 m0, s54, 0x1c00\n"
    "s_add_u32 s71, s32, 0x19000\n"
    "buffer_load_dwordx4 v0, s[4:7], s71 offen lds\n"
    "s_mov_b32 s70, 0xffff0000\n"
    "s_call_b64 s[56:57], .Lown_%=\n"
    "s_waitcnt vmcnt(0)\n"
    ".Lhw_%=:\n"
    "s_or_b32 s88, s66, s62\n"
    "s_andn2_b32 s68, s88, 0x800\n"
    "s_add_u32 s34, s43, 0x0\n"
    "s_mov_b32 s64, 0\n"
    "s_mov_b32 s33, 2\n"
    "s_movk_i32 s70, 0\n"
    "s_mov_b32 s55, 1\n"
    "s_mov_b64 s[4:5], s[14:15]\n"
    "s_mov_b32 s6, 0x400000\n"
    "s_call_b64 s[56:57], .Lwo_%=\n"
    "s_add_u32 s34, s43, 0x0\n"
    "s_mov_b32 s64, 0\n"
    "s_mov_b32 s33, 0\n"
    "s_movk_i32 s70, 1100\n"
    "s_mov_b32 s55, 1\n"
    "s_mov_b64 s[4:5], s[18:19]\n"
    "s_mov_b32 s6, 0x400000\n"
    "s_call_b64 s[56:57], .Lrq_%=\n"
    "s_addk_i32 s90, 32\n"
    "s_swappc_b64 s[96:97], s[94:95]\n"
    "s_swappc_b64 s[96:97], s[94:95]\n"
    "s_swappc_b64 s[96:97], s[94:95]\n"
    "s_swappc_b64 s[96:97], s[94:95]\n"
    "s_swappc_b64 s[96:97], s[94:95]\n"
    "s_add_u32 s34, s43, 0x34000\n"
    "s_mov_b32 s55, 0\n"
    "s_lshr_b32 s42, s62, 5\n"
    "s_mov_b32 s35, 1\n"
    "s_movk_i32 s36, 64\n"
    "s_mov_b32 s47, 0\n"
    "s_mov_b32 s64, -1\n"
    "s_movk_i32 s70, 0\n"
    "s_call_b64 s[56:57], .Latt1_%=\n"
    "s_or_b32 s88, s66, s61\n"
    "s_add_u32 s88, s88, 0x2000000\n"
    "s_andn2_b32 s68, s88, 0x800\n"
    "s_add_u32 s34, s43, 0x34000\n"
    "s_mov_b32 s64, 0\n"
    "s_mov_b32 s33, 1\n"
    "s_movk_i32 s70, 0\n"
    "s_mov_b32 s55, 1\n"
    "s_mov_b64 s[4:5], s[12:13]\n"
    "s_mov_b32 s6, 0x400000\n"
    "s_call_b64 s[56:57], .Lwo_%=\n"
    "s_add_u32 s34, s43, 0x34000\n"
    "s_mov_b32 s64, 0\n"
    "s_mov_b32 s33, 0\n"
    "s_movk_i32 s70, 1100\n"
    "s_mov_b32 s55, 1\n"
    "s_mov_b64 s[4:5], s[16:17]\n"
    "s_mov_b32 s6, 0x400000\n"
    "s_call_b64 s[56:57], .Lrq_%=\n"
    "s_add_u32 s34, s43, 0x68000\n"
    "s_mov_b32 s55, 0\n"
    "s_lshr_b32 s42, s63, 5\n"
    "s_mov_b32 s35, 0\n"
    "s_movk_i32 s36, -1\n"
    "s_mov_b32 s47, 16\n"
    "s_mov_b32 s64, -1\n"
    "s_movk_i32 s70, 750\n"
    "s_call_b64 s[56:57], .Latt_%=\n"
    "s_waitcnt lgkmcnt(0)\n"
    "s_barrier\n"
    "s_add_u32 s34, s43, 0xd0000\n"
    "s_mov_b32 s55, 0\n"
    "s_lshr_b32 s42, s61, 5\n"
    "s_mov_b32 s35, 0\n"
    "s_movk_i32 s36, 96\n"
    "s_mov_b32 s47, 0\n"
    "s_add_u32 s64, s43, 0x68000\n"
    "s_movk_i32 s70, 0\n"
    "s_call_b64 s[56:57], .Latt1_%=\n"
    "s_add_u32 s34, s43, 0x68000\n"
    "s_mov_b32 s64, 0\n"
    "s_mov_b32 s33, 7\n"
    "s_movk_i32 s70, 0\n"
    "s_mov_b32 s55, 0\n"
    "s_call_b64 s[56:57], .Lwo_%=\n"
    "s_or_b32 s88, s66, s62\n"
    "s_add_u32 s88, s88, 0x2000000\n"
    "s_andn2_b32 s68, s88, 0x800\n"
    "s_add_u32 s34, s43, 0xd0000\n"
    "s_mov_b32 s64, 0\n"
    "s_mov_b32 s33, 1\n"
    "s_movk_i32 s70, 30\n"
    "s_mov_b32 s55, 1\n"
    "s_mov_b64 s[4:5], s[14:15]\n"
    "s_mov_b32 s6, 0x400000\n"
    "s_call_b64 s[56:57], .Lwo3_%=\n"
    "s_add_u32 s34, s43, 0x68000\n"
    "s_mov_b32 s64, 0\n"
    "s_mov_b32 s33, 0\n"
    "s_movk_i32 s70, 0\n"
    "s_mov_b32 s55, 0\n"
    "s_call_b64 s[56:57], .Lrq_%=\n"
    "s_waitcnt lgkmcnt(0)\n"
    "s_barrier\n"
    "v_swap_b32 v88, v90\n"
    "v_swap_b32 v89, v91\n"
    "s_add_u32 s34, s43, 0xd0000\n"
    "s_mov_b32 s64, 0\n"
    "s_mov_b32 s33, 0\n"
    "s_movk_i32 s70, 0\n"
    "s_mov_b32 s55, 1\n"
    "s_mov_b64 s[4:5], s[18:19]\n"
    "s_mov_b32 s6, 0x400000\n"
    "s_call_b64 s[56:57], .Lrq_%=\n"
    "s_add_u32 s34, s43, 0x9c000\n"
    "s_mov_b32 s55, 0\n"
    "s_lshr_b32 s42, s62, 5\n"
    "s_mov_b32 s35, 1\n"
    "s_movk_i32 s36, -1\n"
    "s_mov_b32 s47, 16\n"
    "s_mov_b32 s64, -1\n"
    "s_movk_i32 s70, 0\n"
    "s_call_b64 s[56:57], .Latt_%=\n"
    "s_waitcnt lgkmcnt(0)\n"
    "s_barrier\n"
    "s_add_u32 s34, s43, 0x104000\n"
    "s_mov_b32 s55, 0\n"
    "s_lshr_b32 s42, s62, 5\n"
    "s_mov_b32 s35, 1\n"
    "s_movk_i32 s36, 128\n"
    "s_mov_b32 s47, 0\n"
    "s_add_u32 s64, s43, 0x9c000\n"
    "s_movk_i32 s70, 0\n"
    "s_call_b64 s[56:57], .Latt1_%=\n"
    "s_add_u32 s34, s43, 0x9c000\n"
    "s_mov_b32 s64, 0\n"
    "s_mov_b32 s33, 7\n"
    "s_movk_i32 s70, 0\n"
    "s_mov_b32 s55, 0\n"
    "s_call_b64 s[56:57], .Lwo_%=\n"
    "s_add_u32 s34, s43, 0x104000\n"
    "s_mov_b32 s64, 0\n"
    "s_mov_b32 s33, 1\n"
    "s_movk_i32 s70, 30\n"
    "s_mov_b32 s55, 1\n"
    "s_mov_b64 s[4:5], s[12:13]\n"
    "s_mov_b32 s6, 0x400000\n"
    "s_call_b64 s[56:57], .Lwo3_%=\n"
    "s_add_u32 s34, s43, 0x104000\n"
    "s_mov_b32 s64, 0\n"
    "s_mov_b32 s33, 0\n"
    "s_movk_i32 s70, 1100\n"
    "s_cmp_ge_u32 s20, 128\n"
    "s_cselect_b32 s70, 950, s70\n"
    "s_mov_b32 s55, 1\n"
    "s_mov_b64 s[4:5], s[16:17]\n"
    "s_mov_b32 s6, 0x400000\n"
    "s_call_b64 s[56:57], .Lrq_%=\n"
    "s_add_u32 s34, s43, 0x138000\n"
    "s_mov_b32 s55, 0\n"
    "s_lshr_b32 s42, s61, 5\n"
    "s_mov_b32 s35, 0\n"
    "s_movk_i32 s36, -1\n"
    "s_mov_b32 s47, 0\n"
    "s_mov_b32 s64, -1\n"
    "s_movk_i32 s70, 750\n"
    "s_cmp_ge_u32 s20, 128\n"
    "s_cselect_b32 s70, 750, s70\n"
    "s_call_b64 s[56:57], .Latt_%=\n"
    "s_add_u32 s34, s43, 0x138000\n"
    "s_mov_b32 s64, 0\n"
    "s_mov_b32 s33, 0\n"
    "s_movk_i32 s70, 600\n"
    "s_cmp_ge_u32 s20, 128\n"
    "s_cselect_b32 s70, 550, s70\n"
    "s_mov_b32 s55, 1\n"
    "s_mov_b64 s[4:5], s[14:15]\n"
    "s_mov_b32 s6, 0x400000\n"
    "s_call_b64 s[56:57], .Lwo_%=\n"
    "s_add_u32 s34, s43, 0x138000\n"
    "s_mov_b32 s64, 0\n"
    "s_mov_b32 s33, 0\n"
    "s_movk_i32 s70, 1100\n"
    "s_cmp_ge_u32 s20, 128\n"
    "s_cselect_b32 s70, 950, s70\n"
    "s_mov_b32 s55, 1\n"
    "s_mov_b64 s[4:5], s[18:19]\n"
    "s_mov_b32 s6, 0x400000\n"
    "s_call_b64 s[56:57], .Lrq_%=\n"
    "s_add_u32 s34, s43, 0x16c000\n"
    "s_mov_b32 s55, 0\n"
    "s_lshr_b32 s42, s62, 5\n"
    "s_mov_b32 s35, 1\n"
    "s_movk_i32 s36, -1\n"
    "s_mov_b32 s47, 0\n"
    "s_mov_b32 s64, -1\n"
    "s_movk_i32 s70, 750\n"
    "s_cmp_ge_u32 s20, 128\n"
    "s_cselect_b32 s70, 750, s70\n"
    "s_call_b64 s[56:57], .Latt_%=\n"
    "s_add_u32 s34, s43, 0x16c000\n"
    "s_mov_b32 s64, 0\n"
    "s_mov_b32 s33, 0\n"
    "s_movk_i32 s70, 600\n"
    "s_cmp_ge_u32 s20, 128\n"
    "s_cselect_b32 s70, 550, s70\n"
    "s_mov_b32 s55, 0\n"
    "s_call_b64 s[56:57], .Lwo_%=\n"
    "s_and_b32 s44, s20, 127\n"
    "s_cmp_gt_u32 s44, 1\n"
    "s_cbranch_scc1 .Lend_%=\n"
    "s_cmp_eq_u32 s44, 1\n"
    "s_cbranch_scc1 .L36_%=\n"
    "s_add_u32 s34, s43, 0x16c000\n"
    "s_mov_b32 s64, 0\n"
    "s_mov_b32 s33, 1\n"
    "s_movk_i32 s70, 1100\n"
    "s_cmp_ge_u32 s20, 128\n"
    "s_cselect_b32 s70, 950, s70\n"
    "s_mov_b32 s55, 0\n"
    "s_call_b64 s[56:57], .Lrq_%=\n"
    "s_or_b32 s44, s20, s21\n"
    "s_cmp_lg_u32 s44, 0\n"
    "s_cbranch_scc1 .L38_%=\n"
    "s_mov_b32 s44, 0x804000\n"
    "v_mov_b32 v0, s69\n"
    "s_mov_b64 exec, 1\n"
    "buffer_store_dword v0, off, s[0:3], s44 sc1\n"
    "s_mov_b64 exec, -1\n"
    ".L38_%=:\n"
    "s_movk_i32 s45, 0x1000\n"
    ".L37_%=:\n"
    "s_mov_b64 s[4:5], s[10:11]\n"
    "s_mov_b32 s6, 0x2000\n"
    "s_lshr_b32 s44, s20, 7\n"
    "s_lshl_b32 s44, s44, 11\n"
    "s_add_u32 s44, s44, s45\n"
    "v_lshrrev_b32 v0, 16, v86\n"
    "buffer_store_dwordx2 v[88:89], v0, s[4:7], s44 offen\n"
    "s_branch .Lend_%=\n"
    ".L36_%=:\n"
    "v_swap_b32 v88, v90\n"
    "v_swap_b32 v89, v91\n"
    "s_add_u32 s34, s43, 0x9c000\n"
    "s_mov_b32 s64, 0\n"
    "s_mov_b32 s33, 1\n"
    "s_movk_i32 s70, 0\n"
    "s_mov_b32 s55, 0\n"
    "s_call_b64 s[56:57], .Lrq_%=\n"
    "s_mov_b32 s45, 0\n"
    "s_branch .L37_%=\n"
    ".Latt_%=:\n"
    "v_and_b32 v18, 0xffff, v81\n"
    "s_add_u32 s45, s34, s22\n"
    "s_lshl_b32 s44, s35, 13\n"
    "s_add_u32 s44, s44, s92\n"
    "v_add_u32 v37, s44, v95\n"
    "ds_read_b128 v[40:43], v37 offset:0\n"
    "ds_read_b128 v[44:47], v37 offset:1024\n"
    "ds_read_b128 v[48:51], v37 offset:2048\n"
    "ds_read_b128 v[52:55], v37 offset:3072\n"
    "ds_read_b128 v[56:59], v37 offset:4096\n"
    "ds_read_b128 v[60:63], v37 offset:5120\n"
    "ds_read_b128 v[64:67], v37 offset:6144\n"
    "ds_read_b128 v[68:71], v37 offset:7168\n"
    "s_waitcnt lgkmcnt(0)\n"
    "s_add_u32 s99, s58, s70\n"
    ".L42_%=:\n"
    "s_memtime s[100:101]\n"
    "s_waitcnt lgkmcnt(0)\n"
    "s_sub_u32 s98, s100, s99\n"
    "s_cmp_lt_i32 s98, 0\n"
    "s_cbranch_scc1 .L42_%=\n"
    "buffer_load_dwordx4 v[0:3], v18, s[0:3], s45 offen sc1\n"
    "buffer_load_dwordx4 v[4:7], v18, s[0:3], s45 offen offset:256 sc1\n"
    "v_perm_b32 v8, v44, v40, s38\n"
    "v_perm_b32 v44, v44, v40, s39\n"
    "v_mov_b32 v40, v8\n"
    "v_perm_b32 v8, v53, v49, s38\n"
    "v_perm_b32 v53, v53, v49, s39\n"
    "v_mov_b32 v49, v8\n"
    "v_perm_b32 v8, v62, v58, s38\n"
    "v_perm_b32 v62, v62, v58, s39\n"
    "v_mov_b32 v58, v8\n"
    "v_perm_b32 v8, v71, v67, s38\n"
    "v_perm_b32 v71, v71, v67, s39\n"
    "v_mov_b32 v67, v8\n"
    "v_perm_b32 v8, v45, v41, s38\n"
    "v_perm_b32 v9, v45, v41, s39\n"
    "v_perm_b32 v41, v52, v48, s38\n"
    "v_perm_b32 v45, v52, v48, s39\n"
    "v_mov_b32 v48, v8\n"
    "v_mov_b32 v52, v9\n"
    "v_perm_b32 v8, v46, v42, s38\n"
    "v_perm_b32 v9, v46, v42, s39\n"
    "v_perm_b32 v42, v60, v56, s38\n"
    "v_perm_b32 v46, v60, v56, s39\n"
    "v_mov_b32 v56, v8\n"
    "v_mov_b32 v60, v9\n"
    "v_perm_b32 v8, v47, v43, s38\n"
    "v_perm_b32 v9, v47, v43, s39\n"
    "v_perm_b32 v43, v68, v64, s38\n"
    "v_perm_b32 v47, v68, v64, s39\n"
    "v_mov_b32 v64, v8\n"
    "v_mov_b32 v68, v9\n"
    "v_perm_b32 v8, v54, v50, s38\n"
    "v_perm_b32 v9, v54, v50, s39\n"
    "v_perm_b32 v50, v61, v57, s38\n"
    "v_perm_b32 v54, v61, v57, s39\n"
    "v_mov_b32 v57, v8\n"
    "v_mov_b32 v61, v9\n"
    "v_perm_b32 v8, v55, v51, s38\n"
    "v_perm_b32 v9, v55, v51, s39\n"
    "v_perm_b32 v51, v69, v65, s38\n"
    "v_perm_b32 v55, v69, v65, s39\n"
    "v_mov_b32 v65, v8\n"
    "v_mov_b32 v69, v9\n"
    "v_perm_b32 v8, v63, v59, s38\n"
    "v_perm_b32 v9, v63, v59, s39\n"
    "v_perm_b32 v59, v70, v66, s38\n"
    "v_perm_b32 v63, v70, v66, s39\n"
    "v_mov_b32 v66, v8\n"
    "v_mov_b32 v70, v9\n"
    "s_cmp_lt_i32 s89, s90\n"
    "s_cbranch_scc1 .L43_%=\n"
    "s_waitcnt vmcnt(0)\n"
    "s_mov_b32 s91, s89\n"
    ".L44_%=:\n"
    ".Latt2_%=:\n"
    "s_cmp_lg_u32 s35, 0\n"
    "s_cbranch_scc1 .L40_%=\n"
    "v_mfma_f32_16x16x32_bf16 v[20:23], a[0:3], v[0:3], 0\n"
    "v_max3_u32 v19, v0, v1, v2\n"
    "v_max3_u32 v19, v19, v3, v4\n"
    "v_mfma_f32_16x16x32_bf16 v[20:23], a[4:7], v[4:7], v[20:23]\n"
    "v_max3_u32 v19, v19, v5, v6\n"
    "v_max_u32 v19, v19, v7\n"
    "v_mfma_f32_16x16x32_bf16 v[24:27], a[8:11], v[0:3], 0\n"
    "v_mfma_f32_16x16x32_bf16 v[24:27], a[12:15], v[4:7], v[24:27]\n"
    "v_cmp_eq_u32 vcc, -1, v19\n"
    "s_cbranch_vccnz .L52_%=\n"
    ".L51_%=:\n"
    "s_lshl_b32 s71, s21, 5\n"
    "v_mfma_f32_16x16x32_bf16 v[28:31], a[16:19], v[0:3], 0\n"
    "v_mfma_f32_16x16x32_bf16 v[28:31], a[20:23], v[4:7], v[28:31]\n"
    "v_add_f32_dpp v8, v21, v20 row_ror:4 row_mask:0xf bank_mask:0x1\n"
    "v_add_f32_dpp v8, v22, v23 row_ror:4 row_mask:0xf bank_mask:0x2\n"
    "v_lshrrev_b32 v72, 16, v81\n"
    "v_mfma_f32_16x16x32_bf16 v[20:23], a[24:27], v[0:3], 0\n"
    "v_mfma_f32_16x16x32_bf16 v[20:23], a[28:31], v[4:7], v[20:23]\n"
    "v_add_f32_dpp v8, v25, v24 row_ror:4 row_mask:0xf bank_mask:0x4\n"
    "v_add_f32_dpp v8, v26, v27 row_ror:4 row_mask:0xf bank_mask:0x8\n"
    "v_and_b32 v73, 0xffff, v82\n"
    "s_cmp_gt_i32 s47, 0\n"
    "s_cbranch_scc1 .L53_%=\n"
    ".L54_%=:\n"
    "v_mfma_f32_16x16x32_bf16 v[24:27], a[32:35], v[0:3], 0\n"
    "v_mfma_f32_16x16x32_bf16 v[24:27], a[36:39], v[4:7], v[24:27]\n"
    "v_add_f32_dpp v9, v29, v28 row_ror:4 row_mask:0xf bank_mask:0x1\n"
    "v_add_f32_dpp v9, v30, v31 row_ror:4 row_mask:0xf bank_mask:0x2\n"
    "v_lshrrev_b32 v74, 16, v82\n"
    "v_mfma_f32_16x16x32_bf16 v[28:31], a[40:43], v[0:3], 0\n"
    "v_mfma_f32_16x16x32_bf16 v[28:31], a[44:47], v[4:7], v[28:31]\n"
    "v_add_f32_dpp v9, v21, v20 row_ror:4 row_mask:0xf bank_mask:0x4\n"
    "v_add_f32_dpp v9, v22, v23 row_ror:4 row_mask:0xf bank_mask:0x8\n"
    "v_mov_b32 v75, s71\n"
    "v_mfma_f32_16x16x32_bf16 v[20:23], a[48:51], v[0:3], 0\n"
    "v_mfma_f32_16x16x32_bf16 v[20:23], a[52:55], v[4:7], v[20:23]\n"
    "v_add_f32_dpp v10, v25, v24 row_ror:4 row_mask:0xf bank_mask:0x1\n"
    "v_add_f32_dpp v10, v26, v27 row_ror:4 row_mask:0xf bank_mask:0x2\n"
    "v_and_b32 v76, 0xffff, v83\n"
    "v_mfma_f32_16x16x32_bf16 v[24:27], a[56:59], v[0:3], 0\n"
    "v_mfma_f32_16x16x32_bf16 v[24:27], a[60:63], v[4:7], v[24:27]\n"
    "v_add_f32_dpp v10, v29, v28 row_ror:4 row_mask:0xf bank_mask:0x4\n"
    "v_add_f32_dpp v10, v30, v31 row_ror:4 row_mask:0xf bank_mask:0x8\n"
    "s_cmp_gt_i32 s47, 0\n"
    "s_cbranch_scc1 .L55_%=\n"
    ".L56_%=:\n"
    "s_cmp_lt_i32 s89, s90\n"
    "s_cbranch_scc1 .L57_%=\n"
    ".L58_%=:\n"
    "v_mfma_f32_16x16x32_bf16 v[28:31], a[64:67], v[0:3], 0\n"
    "v_mfma_f32_16x16x32_bf16 v[28:31], a[68:71], v[4:7], v[28:31]\n"
    "v_add_f32_dpp v11, v21, v20 row_ror:4 row_mask:0xf bank_mask:0x1\n"
    "v_add_f32_dpp v11, v22, v23 row_ror:4 row_mask:0xf bank_mask:0x2\n"
    "v_max3_f32 v16, v8, v9, v10\n"
    "v_mfma_f32_16x16x32_bf16 v[20:23], a[72:75], v[0:3], 0\n"
    "v_mfma_f32_16x16x32_bf16 v[20:23], a[76:79], v[4:7], v[20:23]\n"
    "v_add_f32_dpp v11, v25, v24 row_ror:4 row_mask:0xf bank_mask:0x4\n"
    "v_add_f32_dpp v11, v26, v27 row_ror:4 row_mask:0xf bank_mask:0x8\n"
    "v_mfma_f32_16x16x32_bf16 v[24:27], a[80:83], v[0:3], 0\n"
    "v_mfma_f32_16x16x32_bf16 v[24:27], a[84:87], v[4:7], v[24:27]\n"
    "v_add_f32_dpp v12, v29, v28 row_ror:4 row_mask:0xf bank_mask:0x1\n"
    "v_add_f32_dpp v12, v30, v31 row_ror:4 row_mask:0xf bank_mask:0x2\n"
    "v_mfma_f32_16x16x32_bf16 v[28:31], a[88:91], v[0:3], 0\n"
    "v_mfma_f32_16x16x32_bf16 v[28:31], a[92:95], v[4:7], v[28:31]\n"
    "v_add_f32_dpp v12, v21, v20 row_ror:4 row_mask:0xf bank_mask:0x4\n"
    "v_add_f32_dpp v12, v22, v23 row_ror:4 row_mask:0xf bank_mask:0x8\n"
    "v_mfma_f32_16x16x32_bf16 v[20:23], a[96:99], v[0:3], 0\n"
    "v_mfma_f32_16x16x32_bf16 v[20:23], a[100:103], v[4:7], v[20:23]\n"
    "v_add_f32_dpp v13, v25, v24 row_ror:4 row_mask:0xf bank_mask:0x1\n"
    "v_add_f32_dpp v13, v26, v27 row_ror:4 row_mask:0xf bank_mask:0x2\n"
    "v_max3_f32 v16, v16, v11, v12\n"
    "v_mfma_f32_16x16x32_bf16 v[24:27], a[104:107], v[0:3], 0\n"
    "v_mfma_f32_16x16x32_bf16 v[24:27], a[108:111], v[4:7], v[24:27]\n"
    "v_add_f32_dpp v13, v29, v28 row_ror:4 row_mask:0xf bank_mask:0x4\n"
    "v_add_f32_dpp v13, v30, v31 row_ror:4 row_mask:0xf bank_mask:0x8\n"
    "v_mfma_f32_16x16x32_bf16 v[28:31], a[112:115], v[0:3], 0\n"
    "v_mfma_f32_16x16x32_bf16 v[28:31], a[116:119], v[4:7], v[28:31]\n"
    "v_add_f32_dpp v14, v21, v20 row_ror:4 row_mask:0xf bank_mask:0x1\n"
    "v_add_f32_dpp v14, v22, v23 row_ror:4 row_mask:0xf bank_mask:0x2\n"
    "v_mfma_f32_16x16x32_bf16 v[20:23], a[120:123], v[0:3], 0\n"
    "v_mfma_f32_16x16x32_bf16 v[20:23], a[124:127], v[4:7], v[20:23]\n"
    "v_add_f32_dpp v14, v25, v24 row_ror:4 row_mask:0xf bank_mask:0x4\n"
    "v_add_f32_dpp v14, v26, v27 row_ror:4 row_mask:0xf bank_mask:0x8\n"
    "s_cmp_lt_i32 s89, s90\n"
    "s_cbranch_scc1 .L59_%=\n"
    ".L60_%=:\n"
    "v_max3_f32 v16, v16, v13, v14\n"
    "v_mov_b32 v24, 0\n"
    "v_mov_b32 v25, 0\n"
    "v_add_f32_dpp v15, v29, v28 row_ror:4 row_mask:0xf bank_mask:0x1\n"
    "v_add_f32_dpp v15, v30, v31 row_ror:4 row_mask:0xf bank_mask:0x2\n"
    "v_mov_b32 v26, 0\n"
    "v_mov_b32 v27, 0\n"
    "v_mov_b32 v28, 0\n"
    "v_add_f32_dpp v15, v21, v20 row_ror:4 row_mask:0xf bank_mask:0x4\n"
    "v_add_f32_dpp v15, v22, v23 row_ror:4 row_mask:0xf bank_mask:0x8\n"
    "v_max_f32 v16, v16, v15\n"
    "v_mov_b32 v29, 0\n"
    "v_mov_b32 v30, 0\n"
    "v_max_f32_dpp v16, v16, v16 row_ror:4 row_mask:0xf bank_mask:0xf\n"
    "v_mov_b32 v31, 0\n"
    "v_mov_b32 v20, 0x3f803f80\n"
    "v_max_f32_dpp v16, v16, v16 row_ror:8 row_mask:0xf bank_mask:0xf\n"
    "v_mov_b32 v17, v16\n"
    "v_mov_b32 v21, 0x3f803f80\n"
    "v_mov_b32 v22, 0x3f803f80\n"
    "v_permlane16_swap_b32 v16, v17\n"
    "v_max_f32 v16, v16, v17\n"
    "v_mov_b32 v17, v16\n"
    "v_mov_b32 v23, 0x3f803f80\n"
    "s_nop 0\n"
    "v_permlane32_swap_b32 v16, v17\n"
    "v_max_f32 v16, v16, v17\n"
    "v_mul_f32 v36, s40, v16\n"
    "v_fma_f32 v8, v8, s40, -v36\n"
    "v_fma_f32 v9, v9, s40, -v36\n"
    "v_fma_f32 v10, v10, s40, -v36\n"
    "v_fma_f32 v11, v11, s40, -v36\n"
    "v_fma_f32 v12, v12, s40, -v36\n"
    "v_fma_f32 v13, v13, s40, -v36\n"
    "v_fma_f32 v14, v14, s40, -v36\n"
    "v_fma_f32 v15, v15, s40, -v36\n"
    "v_exp_f32 v8, v8\n"
    "v_exp_f32 v9, v9\n"
    "v_exp_f32 v10, v10\n"
    "v_exp_f32 v11, v11\n"
    "v_exp_f32 v12, v12\n"
    "v_exp_f32 v13, v13\n"
    "v_exp_f32 v14, v14\n"
    "v_exp_f32 v15, v15\n"
    "v_cvt_pk_bf16_f32 v32, v8, v9\n"
    "v_cvt_pk_bf16_f32 v33, v10, v11\n"
    "v_cvt_pk_bf16_f32 v34, v12, v13\n"
    "v_cvt_pk_bf16_f32 v35, v14, v15\n"
    "s_cmp_lt_i32 s89, s90\n"
    "s_cbranch_scc1 .L61_%=\n"
    ".L62_%=:\n"
    "s_lshr_b32 s98, s47, 1\n"
    "s_add_i32 s90, s90, s98\n"
    "v_mov_b32_dpp v24, v32 quad_perm:[0,1,2,3] row_mask:0xf bank_mask:0x1\n"
    "v_mov_b32_dpp v25, v32 row_shl:4 row_mask:0xf bank_mask:0x1\n"
    "v_mov_b32_dpp v26, v32 row_shl:8 row_mask:0xf bank_mask:0x1\n"
    "v_mov_b32_dpp v27, v32 row_shl:12 row_mask:0xf bank_mask:0x1\n"
    "s_cmp_ge_i32 s36, 0\n"
    "s_cbranch_scc1 .L65_%=\n"
    ".L66_%=:\n"
    "v_mfma_f32_16x16x32_bf16 v[0:3], v[40:43], v[24:27], 0\n"
    "v_mov_b32_dpp v28, v32 row_shr:4 row_mask:0xf bank_mask:0x2\n"
    "v_mov_b32_dpp v29, v32 quad_perm:[0,1,2,3] row_mask:0xf bank_mask:0x2\n"
    "v_mfma_f32_16x16x32_bf16 v[4:7], v[48:51], v[24:27], 0\n"
    "v_mov_b32_dpp v30, v32 row_shl:4 row_mask:0xf bank_mask:0x2\n"
    "v_mov_b32_dpp v31, v32 row_shl:8 row_mask:0xf bank_mask:0x2\n"
    "v_mfma_f32_16x16x32_bf16 v[8:11], v[56:59], v[24:27], 0\n"
    "v_mfma_f32_16x16x32_bf16 v[12:15], v[64:67], v[24:27], 0\n"
    "v_mfma_f32_16x16x32_bf16 v[16:19], v[20:23], v[24:27], 0\n"
    "v_mfma_f32_16x16x32_bf16 v[0:3], v[44:47], v[28:31], v[0:3]\n"
    "v_mov_b32_dpp v24, v33 quad_perm:[0,1,2,3] row_mask:0xf bank_mask:0x1\n"
    "v_mov_b32_dpp v25, v33 row_shl:4 row_mask:0xf bank_mask:0x1\n"
    "v_mfma_f32_16x16x32_bf16 v[4:7], v[52:55], v[28:31], v[4:7]\n"
    "v_mov_b32_dpp v26, v33 row_shl:8 row_mask:0xf bank_mask:0x1\n"
    "v_mov_b32_dpp v27, v33 row_shl:12 row_mask:0xf bank_mask:0x1\n"
    "v_mfma_f32_16x16x32_bf16 v[8:11], v[60:63], v[28:31], v[8:11]\n"
    "v_mfma_f32_16x16x32_bf16 v[12:15], v[68:71], v[28:31], v[12:15]\n"
    "s_cmp_ge_i32 s36, 0\n"
    "s_cbranch_scc1 .L70_%=\n"
    ".L71_%=:\n"
    "v_mfma_f32_16x16x32_bf16 v[0:3], v[160:163], v[24:27], v[0:3]\n"
    "v_mov_b32_dpp v28, v33 row_shr:4 row_mask:0xf bank_mask:0x2\n"
    "v_mov_b32_dpp v29, v33 quad_perm:[0,1,2,3] row_mask:0xf bank_mask:0x2\n"
    "v_mfma_f32_16x16x32_bf16 v[4:7], v[168:171], v[24:27], v[4:7]\n"
    "v_mov_b32_dpp v30, v33 row_shl:4 row_mask:0xf bank_mask:0x2\n"
    "v_mov_b32_dpp v31, v33 row_shl:8 row_mask:0xf bank_mask:0x2\n"
    "v_mfma_f32_16x16x32_bf16 v[8:11], v[176:179], v[24:27], v[8:11]\n"
    "v_mfma_f32_16x16x32_bf16 v[12:15], v[184:187], v[24:27], v[12:15]\n"
    "v_mfma_f32_16x16x32_bf16 v[16:19], v[20:23], v[24:27], v[16:19]\n"
    "v_mfma_f32_16x16x32_bf16 v[0:3], v[164:167], v[28:31], v[0:3]\n"
    "v_mov_b32_dpp v24, v34 quad_perm:[0,1,2,3] row_mask:0xf bank_mask:0x1\n"
    "v_mov_b32_dpp v25, v34 row_shl:4 row_mask:0xf bank_mask:0x1\n"
    "v_mfma_f32_16x16x32_bf16 v[4:7], v[172:175], v[28:31], v[4:7]\n"
    "v_mov_b32_dpp v26, v34 row_shl:8 row_mask:0xf bank_mask:0x1\n"
    "v_mov_b32_dpp v27, v34 row_shl:12 row_mask:0xf bank_mask:0x1\n"
    "v_mfma_f32_16x16x32_bf16 v[8:11], v[180:183], v[28:31], v[8:11]\n"
    "v_mfma_f32_16x16x32_bf16 v[12:15], v[188:191], v[28:31], v[12:15]\n"
    "s_cmp_lt_i32 s89, s90\n"
    "s_cbranch_scc1 .L80_%=\n"
    ".L81_%=:\n"
    "s_cmp_ge_i32 s36, 0\n"
    "s_cbranch_scc1 .L82_%=\n"
    ".L83_%=:\n"
    "v_mfma_f32_16x16x32_bf16 v[0:3], v[192:195], v[24:27], v[0:3]\n"
    "v_mov_b32_dpp v28, v34 row_shr:4 row_mask:0xf bank_mask:0x2\n"
    "v_mov_b32_dpp v29, v34 quad_perm:[0,1,2,3] row_mask:0xf bank_mask:0x2\n"
    "v_mfma_f32_16x16x32_bf16 v[4:7], v[200:203], v[24:27], v[4:7]\n"
    "v_mov_b32_dpp v30, v34 row_shl:4 row_mask:0xf bank_mask:0x2\n"
    "v_mov_b32_dpp v31, v34 row_shl:8 row_mask:0xf bank_mask:0x2\n"
    "v_mfma_f32_16x16x32_bf16 v[8:11], v[208:211], v[24:27], v[8:11]\n"
    "v_mfma_f32_16x16x32_bf16 v[12:15], v[216:219], v[24:27], v[12:15]\n"
    "v_mfma_f32_16x16x32_bf16 v[16:19], v[20:23], v[24:27], v[16:19]\n"
    "v_mfma_f32_16x16x32_bf16 v[0:3], v[196:199], v[28:31], v[0:3]\n"
    "v_mov_b32_dpp v24, v35 quad_perm:[0,1,2,3] row_mask:0xf bank_mask:0x1\n"
    "v_mov_b32_dpp v25, v35 row_shl:4 row_mask:0xf bank_mask:0x1\n"
    "v_mfma_f32_16x16x32_bf16 v[4:7], v[204:207], v[28:31], v[4:7]\n"
    "v_mov_b32_dpp v26, v35 row_shl:8 row_mask:0xf bank_mask:0x1\n"
    "v_mov_b32_dpp v27, v35 row_shl:12 row_mask:0xf bank_mask:0x1\n"
    "v_mfma_f32_16x16x32_bf16 v[8:11], v[212:215], v[28:31], v[8:11]\n"
    "v_mfma_f32_16x16x32_bf16 v[12:15], v[220:223], v[28:31], v[12:15]\n"
    "s_cmp_ge_i32 s36, 0\n"
    "s_cbranch_scc1 .L85_%=\n"
    ".L86_%=:\n"
    "v_mfma_f32_16x16x32_bf16 v[0:3], v[224:227], v[24:27], v[0:3]\n"
    "v_mov_b32_dpp v28, v35 row_shr:4 row_mask:0xf bank_mask:0x2\n"
    "v_mov_b32_dpp v29, v35 quad_perm:[0,1,2,3] row_mask:0xf bank_mask:0x2\n"
    "v_mfma_f32_16x16x32_bf16 v[4:7], v[232:235], v[24:27], v[4:7]\n"
    "v_mov_b32_dpp v30, v35 row_shl:4 row_mask:0xf bank_mask:0x2\n"
    "v_mov_b32_dpp v31, v35 row_shl:8 row_mask:0xf bank_mask:0x2\n"
    "v_mfma_f32_16x16x32_bf16 v[8:11], v[240:243], v[24:27], v[8:11]\n"
    "v_mfma_f32_16x16x32_bf16 v[12:15], v[248:251], v[24:27], v[12:15]\n"
    "v_mfma_f32_16x16x32_bf16 v[16:19], v[20:23], v[24:27], v[16:19]\n"
    "v_mfma_f32_16x16x32_bf16 v[0:3], v[228:231], v[28:31], v[0:3]\n"
    "v_mfma_f32_16x16x32_bf16 v[4:7], v[236:239], v[28:31], v[4:7]\n"
    "v_mfma_f32_16x16x32_bf16 v[8:11], v[244:247], v[28:31], v[8:11]\n"
    "v_mfma_f32_16x16x32_bf16 v[12:15], v[252:255], v[28:31], v[12:15]\n"
    "s_branch .L41_%=\n"
    ".L40_%=:\n"
    "v_mfma_f32_16x16x32_bf16 v[20:23], a[128:131], v[0:3], 0\n"
    "v_max3_u32 v19, v0, v1, v2\n"
    "v_max3_u32 v19, v19, v3, v4\n"
    "v_mfma_f32_16x16x32_bf16 v[20:23], a[132:135], v[4:7], v[20:23]\n"
    "v_max3_u32 v19, v19, v5, v6\n"
    "v_max_u32 v19, v19, v7\n"
    "v_mfma_f32_16x16x32_bf16 v[24:27], a[136:139], v[0:3], 0\n"
    "v_mfma_f32_16x16x32_bf16 v[24:27], a[140:143], v[4:7], v[24:27]\n"
    "v_cmp_eq_u32 vcc, -1, v19\n"
    "s_cbranch_vccnz .L90_%=\n"
    ".L89_%=:\n"
    "s_lshl_b32 s71, s21, 5\n"
    "v_mfma_f32_16x16x32_bf16 v[28:31], a[144:147], v[0:3], 0\n"
    "v_mfma_f32_16x16x32_bf16 v[28:31], a[148:151], v[4:7], v[28:31]\n"
    "v_add_f32_dpp v8, v21, v20 row_ror:4 row_mask:0xf bank_mask:0x1\n"
    "v_add_f32_dpp v8, v22, v23 row_ror:4 row_mask:0xf bank_mask:0x2\n"
    "v_lshrrev_b32 v72, 16, v81\n"
    "v_mfma_f32_16x16x32_bf16 v[20:23], a[152:155], v[0:3], 0\n"
    "v_mfma_f32_16x16x32_bf16 v[20:23], a[156:159], v[4:7], v[20:23]\n"
    "v_add_f32_dpp v8, v25, v24 row_ror:4 row_mask:0xf bank_mask:0x4\n"
    "v_add_f32_dpp v8, v26, v27 row_ror:4 row_mask:0xf bank_mask:0x8\n"
    "v_and_b32 v73, 0xffff, v82\n"
    "s_cmp_gt_i32 s47, 0\n"
    "s_cbranch_scc1 .L91_%=\n"
    ".L92_%=:\n"
    "v_mfma_f32_16x16x32_bf16 v[24:27], a[160:163], v[0:3], 0\n"
    "v_mfma_f32_16x16x32_bf16 v[24:27], a[164:167], v[4:7], v[24:27]\n"
    "v_add_f32_dpp v9, v29, v28 row_ror:4 row_mask:0xf bank_mask:0x1\n"
    "v_add_f32_dpp v9, v30, v31 row_ror:4 row_mask:0xf bank_mask:0x2\n"
    "v_lshrrev_b32 v74, 16, v82\n"
    "v_mfma_f32_16x16x32_bf16 v[28:31], a[168:171], v[0:3], 0\n"
    "v_mfma_f32_16x16x32_bf16 v[28:31], a[172:175], v[4:7], v[28:31]\n"
    "v_add_f32_dpp v9, v21, v20 row_ror:4 row_mask:0xf bank_mask:0x4\n"
    "v_add_f32_dpp v9, v22, v23 row_ror:4 row_mask:0xf bank_mask:0x8\n"
    "v_mov_b32 v75, s71\n"
    "v_mfma_f32_16x16x32_bf16 v[20:23], a[176:179], v[0:3], 0\n"
    "v_mfma_f32_16x16x32_bf16 v[20:23], a[180:183], v[4:7], v[20:23]\n"
    "v_add_f32_dpp v10, v25, v24 row_ror:4 row_mask:0xf bank_mask:0x1\n"
    "v_add_f32_dpp v10, v26, v27 row_ror:4 row_mask:0xf bank_mask:0x2\n"
    "v_and_b32 v76, 0xffff, v83\n"
    "v_mfma_f32_16x16x32_bf16 v[24:27], a[184:187], v[0:3], 0\n"
    "v_mfma_f32_16x16x32_bf16 v[24:27], a[188:191], v[4:7], v[24:27]\n"
    "v_add_f32_dpp v10, v29, v28 row_ror:4 row_mask:0xf bank_mask:0x4\n"
    "v_add_f32_dpp v10, v30, v31 row_ror:4 row_mask:0xf bank_mask:0x8\n"
    "s_cmp_gt_i32 s47, 0\n"
    "s_cbranch_scc1 .L93_%=\n"
    ".L94_%=:\n"
    "s_cmp_lt_i32 s89, s90\n"
    "s_cbranch_scc1 .L95_%=\n"
    ".L96_%=:\n"
    "v_mfma_f32_16x16x32_bf16 v[28:31], a[192:195], v[0:3], 0\n"
    "v_mfma_f32_16x16x32_bf16 v[28:31], a[196:199], v[4:7], v[28:31]\n"
    "v_add_f32_dpp v11, v21, v20 row_ror:4 row_mask:0xf bank_mask:0x1\n"
    "v_add_f32_dpp v11, v22, v23 row_ror:4 row_mask:0xf bank_mask:0x2\n"
    "v_max3_f32 v16, v8, v9, v10\n"
    "v_mfma_f32_16x16x32_bf16 v[20:23], a[200:203], v[0:3], 0\n"
    "v_mfma_f32_16x16x32_bf16 v[20:23], a[204:207], v[4:7], v[20:23]\n"
    "v_add_f32_dpp v11, v25, v24 row_ror:4 row_mask:0xf bank_mask:0x4\n"
    "v_add_f32_dpp v11, v26, v27 row_ror:4 row_mask:0xf bank_mask:0x8\n"
    "v_mfma_f32_16x16x32_bf16 v[24:27], a[208:211], v[0:3], 0\n"
    "v_mfma_f32_16x16x32_bf16 v[24:27], a[212:215], v[4:7], v[24:27]\n"
    "v_add_f32_dpp v12, v29, v28 row_ror:4 row_mask:0xf bank_mask:0x1\n"
    "v_add_f32_dpp v12, v30, v31 row_ror:4 row_mask:0xf bank_mask:0x2\n"
    "v_mfma_f32_16x16x32_bf16 v[28:31], a[216:219], v[0:3], 0\n"
    "v_mfma_f32_16x16x32_bf16 v[28:31], a[220:223], v[4:7], v[28:31]\n"
    "v_add_f32_dpp v12, v21, v20 row_ror:4 row_mask:0xf bank_mask:0x4\n"
    "v_add_f32_dpp v12, v22, v23 row_ror:4 row_mask:0xf bank_mask:0x8\n"
    "v_mfma_f32_16x16x32_bf16 v[20:23], a[224:227], v[0:3], 0\n"
    "v_mfma_f32_16x16x32_bf16 v[20:23], a[228:231], v[4:7], v[20:23]\n"
    "v_add_f32_dpp v13, v25, v24 row_ror:4 row_mask:0xf bank_mask:0x1\n"
    "v_add_f32_dpp v13, v26, v27 row_ror:4 row_mask:0xf bank_mask:0x2\n"
    "v_max3_f32 v16, v16, v11, v12\n"
    "v_mfma_f32_16x16x32_bf16 v[24:27], a[232:235], v[0:3], 0\n"
    "v_mfma_f32_16x16x32_bf16 v[24:27], a[236:239], v[4:7], v[24:27]\n"
    "v_add_f32_dpp v13, v29, v28 row_ror:4 row_mask:0xf bank_mask:0x4\n"
    "v_add_f32_dpp v13, v30, v31 row_ror:4 row_mask:0xf bank_mask:0x8\n"
    "v_mfma_f32_16x16x32_bf16 v[28:31], a[240:243], v[0:3], 0\n"
    "v_mfma_f32_16x16x32_bf16 v[28:31], a[244:247], v[4:7], v[28:31]\n"
    "v_add_f32_dpp v14, v21, v20 row_ror:4 row_mask:0xf bank_mask:0x1\n"
    "v_add_f32_dpp v14, v22, v23 row_ror:4 row_mask:0xf bank_mask:0x2\n"
    "v_mfma_f32_16x16x32_bf16 v[20:23], a[248:251], v[0:3], 0\n"
    "v_mfma_f32_16x16x32_bf16 v[20:23], a[252:255], v[4:7], v[20:23]\n"
    "v_add_f32_dpp v14, v25, v24 row_ror:4 row_mask:0xf bank_mask:0x4\n"
    "v_add_f32_dpp v14, v26, v27 row_ror:4 row_mask:0xf bank_mask:0x8\n"
    "s_cmp_lt_i32 s89, s90\n"
    "s_cbranch_scc1 .L97_%=\n"
    ".L98_%=:\n"
    "v_max3_f32 v16, v16, v13, v14\n"
    "v_mov_b32 v24, 0\n"
    "v_mov_b32 v25, 0\n"
    "v_add_f32_dpp v15, v29, v28 row_ror:4 row_mask:0xf bank_mask:0x1\n"
    "v_add_f32_dpp v15, v30, v31 row_ror:4 row_mask:0xf bank_mask:0x2\n"
    "v_mov_b32 v26, 0\n"
    "v_mov_b32 v27, 0\n"
    "v_mov_b32 v28, 0\n"
    "v_add_f32_dpp v15, v21, v20 row_ror:4 row_mask:0xf bank_mask:0x4\n"
    "v_add_f32_dpp v15, v22, v23 row_ror:4 row_mask:0xf bank_mask:0x8\n"
    "v_max_f32 v16, v16, v15\n"
    "v_mov_b32 v29, 0\n"
    "v_mov_b32 v30, 0\n"
    "v_max_f32_dpp v16, v16, v16 row_ror:4 row_mask:0xf bank_mask:0xf\n"
    "v_mov_b32 v31, 0\n"
    "v_mov_b32 v20, 0x3f803f80\n"
    "v_max_f32_dpp v16, v16, v16 row_ror:8 row_mask:0xf bank_mask:0xf\n"
    "v_mov_b32 v17, v16\n"
    "v_mov_b32 v21, 0x3f803f80\n"
    "v_mov_b32 v22, 0x3f803f80\n"
    "v_permlane16_swap_b32 v16, v17\n"
    "v_max_f32 v16, v16, v17\n"
    "v_mov_b32 v17, v16\n"
    "v_mov_b32 v23, 0x3f803f80\n"
    "s_nop 0\n"
    "v_permlane32_swap_b32 v16, v17\n"
    "v_max_f32 v16, v16, v17\n"
    "v_mul_f32 v36, s40, v16\n"
    "v_fma_f32 v8, v8, s40, -v36\n"
    "v_fma_f32 v9, v9, s40, -v36\n"
    "v_fma_f32 v10, v10, s40, -v36\n"
    "v_fma_f32 v11, v11, s40, -v36\n"
    "v_fma_f32 v12, v12, s40, -v36\n"
    "v_fma_f32 v13, v13, s40, -v36\n"
    "v_fma_f32 v14, v14, s40, -v36\n"
    "v_fma_f32 v15, v15, s40, -v36\n"
    "v_exp_f32 v8, v8\n"
    "v_exp_f32 v9, v9\n"
    "v_exp_f32 v10, v10\n"
    "v_exp_f32 v11, v11\n"
    "v_exp_f32 v12, v12\n"
    "v_exp_f32 v13, v13\n"
    "v_exp_f32 v14, v14\n"
    "v_exp_f32 v15, v15\n"
    "v_cvt_pk_bf16_f32 v32, v8, v9\n"
    "v_cvt_pk_bf16_f32 v33, v10, v11\n"
    "v_cvt_pk_bf16_f32 v34, v12, v13\n"
    "v_cvt_pk_bf16_f32 v35, v14, v15\n"
    "s_cmp_lt_i32 s89, s90\n"
    "s_cbranch_scc1 .L99_%=\n"
    ".L100_%=:\n"
    "s_lshr_b32 s98, s47, 1\n"
    "s_add_i32 s90, s90, s98\n"
    "v_mov_b32_dpp v24, v32 quad_perm:[0,1,2,3] row_mask:0xf bank_mask:0x1\n"
    "v_mov_b32_dpp v25, v32 row_shl:4 row_mask:0xf bank_mask:0x1\n"
    "v_mov_b32_dpp v26, v32 row_shl:8 row_mask:0xf bank_mask:0x1\n"
    "v_mov_b32_dpp v27, v32 row_shl:12 row_mask:0xf bank_mask:0x1\n"
    "s_cmp_ge_i32 s36, 0\n"
    "s_cbranch_scc1 .L103_%=\n"
    ".L104_%=:\n"
    "v_mfma_f32_16x16x32_bf16 v[0:3], v[40:43], v[24:27], 0\n"
    "ds_read_b128 v[40:43], v37 offset:8192\n"
    "v_mov_b32_dpp v28, v32 row_shr:4 row_mask:0xf bank_mask:0x2\n"
    "v_mov_b32_dpp v29, v32 quad_perm:[0,1,2,3] row_mask:0xf bank_mask:0x2\n"
    "v_mfma_f32_16x16x32_bf16 v[4:7], v[48:51], v[24:27], 0\n"
    "ds_read_b128 v[48:51], v37 offset:10240\n"
    "v_mov_b32_dpp v30, v32 row_shl:4 row_mask:0xf bank_mask:0x2\n"
    "v_mov_b32_dpp v31, v32 row_shl:8 row_mask:0xf bank_mask:0x2\n"
    "v_mfma_f32_16x16x32_bf16 v[8:11], v[56:59], v[24:27], 0\n"
    "ds_read_b128 v[56:59], v37 offset:12288\n"
    "v_mfma_f32_16x16x32_bf16 v[12:15], v[64:67], v[24:27], 0\n"
    "ds_read_b128 v[64:67], v37 offset:14336\n"
    "v_mfma_f32_16x16x32_bf16 v[16:19], v[20:23], v[24:27], 0\n"
    "v_mfma_f32_16x16x32_bf16 v[0:3], v[44:47], v[28:31], v[0:3]\n"
    "ds_read_b128 v[44:47], v37 offset:9216\n"
    "v_mov_b32_dpp v24, v33 quad_perm:[0,1,2,3] row_mask:0xf bank_mask:0x1\n"
    "v_mov_b32_dpp v25, v33 row_shl:4 row_mask:0xf bank_mask:0x1\n"
    "v_mfma_f32_16x16x32_bf16 v[4:7], v[52:55], v[28:31], v[4:7]\n"
    "ds_read_b128 v[52:55], v37 offset:11264\n"
    "v_mov_b32_dpp v26, v33 row_shl:8 row_mask:0xf bank_mask:0x1\n"
    "v_mov_b32_dpp v27, v33 row_shl:12 row_mask:0xf bank_mask:0x1\n"
    "v_mfma_f32_16x16x32_bf16 v[8:11], v[60:63], v[28:31], v[8:11]\n"
    "ds_read_b128 v[60:63], v37 offset:13312\n"
    "v_mfma_f32_16x16x32_bf16 v[12:15], v[68:71], v[28:31], v[12:15]\n"
    "ds_read_b128 v[68:71], v37 offset:15360\n"
    "s_waitcnt lgkmcnt(0)\n"
    "v_perm_b32 v78, v44, v40, s38\n"
    "v_perm_b32 v44, v44, v40, s39\n"
    "v_mov_b32 v40, v78\n"
    "v_perm_b32 v78, v53, v49, s38\n"
    "v_perm_b32 v53, v53, v49, s39\n"
    "v_mov_b32 v49, v78\n"
    "v_perm_b32 v78, v62, v58, s38\n"
    "v_perm_b32 v62, v62, v58, s39\n"
    "v_mov_b32 v58, v78\n"
    "v_perm_b32 v78, v71, v67, s38\n"
    "v_perm_b32 v71, v71, v67, s39\n"
    "v_mov_b32 v67, v78\n"
    "v_perm_b32 v78, v45, v41, s38\n"
    "v_perm_b32 v79, v45, v41, s39\n"
    "v_perm_b32 v41, v52, v48, s38\n"
    "v_perm_b32 v45, v52, v48, s39\n"
    "v_mov_b32 v48, v78\n"
    "v_mov_b32 v52, v79\n"
    "v_perm_b32 v78, v46, v42, s38\n"
    "v_perm_b32 v79, v46, v42, s39\n"
    "v_perm_b32 v42, v60, v56, s38\n"
    "v_perm_b32 v46, v60, v56, s39\n"
    "v_mov_b32 v56, v78\n"
    "v_mov_b32 v60, v79\n"
    "v_perm_b32 v78, v47, v43, s38\n"
    "v_perm_b32 v79, v47, v43, s39\n"
    "v_perm_b32 v43, v68, v64, s38\n"
    "v_perm_b32 v47, v68, v64, s39\n"
    "v_mov_b32 v64, v78\n"
    "v_mov_b32 v68, v79\n"
    "v_perm_b32 v78, v54, v50, s38\n"
    "v_perm_b32 v79, v54, v50, s39\n"
    "v_perm_b32 v50, v61, v57, s38\n"
    "v_perm_b32 v54, v61, v57, s39\n"
    "v_mov_b32 v57, v78\n"
    "v_mov_b32 v61, v79\n"
    "v_perm_b32 v78, v55, v51, s38\n"
    "v_perm_b32 v79, v55, v51, s39\n"
    "v_perm_b32 v51, v69, v65, s38\n"
    "v_perm_b32 v55, v69, v65, s39\n"
    "v_mov_b32 v65, v78\n"
    "v_mov_b32 v69, v79\n"
    "v_perm_b32 v78, v63, v59, s38\n"
    "v_perm_b32 v79, v63, v59, s39\n"
    "v_perm_b32 v59, v70, v66, s38\n"
    "v_perm_b32 v63, v70, v66, s39\n"
    "v_mov_b32 v66, v78\n"
    "v_mov_b32 v70, v79\n"
    "v_mfma_f32_16x16x32_bf16 v[0:3], v[40:43], v[24:27], v[0:3]\n"
    "v_mov_b32_dpp v28, v33 row_shr:4 row_mask:0xf bank_mask:0x2\n"
    "v_mov_b32_dpp v29, v33 quad_perm:[0,1,2,3] row_mask:0xf bank_mask:0x2\n"
    "v_mfma_f32_16x16x32_bf16 v[4:7], v[48:51], v[24:27], v[4:7]\n"
    "v_mov_b32_dpp v30, v33 row_shl:4 row_mask:0xf bank_mask:0x2\n"
    "v_mov_b32_dpp v31, v33 row_shl:8 row_mask:0xf bank_mask:0x2\n"
    "v_mfma_f32_16x16x32_bf16 v[8:11], v[56:59], v[24:27], v[8:11]\n"
    "v_mfma_f32_16x16x32_bf16 v[12:15], v[64:67], v[24:27], v[12:15]\n"
    "v_mfma_f32_16x16x32_bf16 v[16:19], v[20:23], v[24:27], v[16:19]\n"
    "v_mfma_f32_16x16x32_bf16 v[0:3], v[44:47], v[28:31], v[0:3]\n"
    "v_mov_b32_dpp v24, v34 quad_perm:[0,1,2,3] row_mask:0xf bank_mask:0x1\n"
    "v_mov_b32_dpp v25, v34 row_shl:4 row_mask:0xf bank_mask:0x1\n"
    "v_mfma_f32_16x16x32_bf16 v[4:7], v[52:55], v[28:31], v[4:7]\n"
    "v_mov_b32_dpp v26, v34 row_shl:8 row_mask:0xf bank_mask:0x1\n"
    "v_mov_b32_dpp v27, v34 row_shl:12 row_mask:0xf bank_mask:0x1\n"
    "v_mfma_f32_16x16x32_bf16 v[8:11], v[60:63], v[28:31], v[8:11]\n"
    "v_mfma_f32_16x16x32_bf16 v[12:15], v[68:71], v[28:31], v[12:15]\n"
    "s_cmp_lt_i32 s89, s90\n"
    "s_cbranch_scc1 .L116_%=\n"
    ".L117_%=:\n"
    "s_cmp_ge_i32 s36, 0\n"
    "s_cbranch_scc1 .L118_%=\n"
    ".L119_%=:\n"
    "v_mfma_f32_16x16x32_bf16 v[0:3], v[96:99], v[24:27], v[0:3]\n"
    "v_mov_b32_dpp v28, v34 row_shr:4 row_mask:0xf bank_mask:0x2\n"
    "v_mov_b32_dpp v29, v34 quad_perm:[0,1,2,3] row_mask:0xf bank_mask:0x2\n"
    "v_mfma_f32_16x16x32_bf16 v[4:7], v[104:107], v[24:27], v[4:7]\n"
    "v_mov_b32_dpp v30, v34 row_shl:4 row_mask:0xf bank_mask:0x2\n"
    "v_mov_b32_dpp v31, v34 row_shl:8 row_mask:0xf bank_mask:0x2\n"
    "v_mfma_f32_16x16x32_bf16 v[8:11], v[112:115], v[24:27], v[8:11]\n"
    "v_mfma_f32_16x16x32_bf16 v[12:15], v[120:123], v[24:27], v[12:15]\n"
    "v_mfma_f32_16x16x32_bf16 v[16:19], v[20:23], v[24:27], v[16:19]\n"
    "v_mfma_f32_16x16x32_bf16 v[0:3], v[100:103], v[28:31], v[0:3]\n"
    "v_mov_b32_dpp v24, v35 quad_perm:[0,1,2,3] row_mask:0xf bank_mask:0x1\n"
    "v_mov_b32_dpp v25, v35 row_shl:4 row_mask:0xf bank_mask:0x1\n"
    "v_mfma_f32_16x16x32_bf16 v[4:7], v[108:111], v[28:31], v[4:7]\n"
    "v_mov_b32_dpp v26, v35 row_shl:8 row_mask:0xf bank_mask:0x1\n"
    "v_mov_b32_dpp v27, v35 row_shl:12 row_mask:0xf bank_mask:0x1\n"
    "v_mfma_f32_16x16x32_bf16 v[8:11], v[116:119], v[28:31], v[8:11]\n"
    "v_mfma_f32_16x16x32_bf16 v[12:15], v[124:127], v[28:31], v[12:15]\n"
    "s_cmp_ge_i32 s36, 0\n"
    "s_cbranch_scc1 .L121_%=\n"
    ".L122_%=:\n"
    "v_mfma_f32_16x16x32_bf16 v[0:3], v[128:131], v[24:27], v[0:3]\n"
    "v_mov_b32_dpp v28, v35 row_shr:4 row_mask:0xf bank_mask:0x2\n"
    "v_mov_b32_dpp v29, v35 quad_perm:[0,1,2,3] row_mask:0xf bank_mask:0x2\n"
    "v_mfma_f32_16x16x32_bf16 v[4:7], v[136:139], v[24:27], v[4:7]\n"
    "v_mov_b32_dpp v30, v35 row_shl:4 row_mask:0xf bank_mask:0x2\n"
    "v_mov_b32_dpp v31, v35 row_shl:8 row_mask:0xf bank_mask:0x2\n"
    "v_mfma_f32_16x16x32_bf16 v[8:11], v[144:147], v[24:27], v[8:11]\n"
    "v_mfma_f32_16x16x32_bf16 v[12:15], v[152:155], v[24:27], v[12:15]\n"
    "v_mfma_f32_16x16x32_bf16 v[16:19], v[20:23], v[24:27], v[16:19]\n"
    "v_mfma_f32_16x16x32_bf16 v[0:3], v[132:135], v[28:31], v[0:3]\n"
    "v_mfma_f32_16x16x32_bf16 v[4:7], v[140:143], v[28:31], v[4:7]\n"
    "v_mfma_f32_16x16x32_bf16 v[8:11], v[148:151], v[28:31], v[8:11]\n"
    "v_mfma_f32_16x16x32_bf16 v[12:15], v[156:159], v[28:31], v[12:15]\n"
    ".L41_%=:\n"
    "s_mov_b64 exec, 15\n"
    "s_nop 2\n"
    "ds_write2_b32 v73, v36, v16 offset1:1\n"
    "s_mov_b64 exec, s[48:49]\n"
    "ds_write_b128 v72, v[0:3] offset:0\n"
    "ds_write_b128 v72, v[4:7] offset:32\n"
    "ds_write_b128 v72, v[8:11] offset:64\n"
    "ds_write_b128 v72, v[12:15] offset:96\n"
    "s_mov_b64 exec, -1\n"
    "s_add_i32 s90, s90, s47\n"
    "s_andn2_b32 s44, s23, 0x100\n"
    "s_andn2_b32 s45, s24, 0x100\n"
    "s_add_u32 s42, s42, s34\n"
    "s_add_u32 s44, s44, s42\n"
    "s_add_u32 s45, s45, s42\n"
    "s_cmp_lt_i32 s89, s90\n"
    "s_cbranch_scc1 .L125_%=\n"
    ".L126_%=:\n"
    "s_waitcnt lgkmcnt(0)\n"
    "s_barrier\n"
    "ds_read_b128 v[0:3], v75 offset:8192\n"
    "ds_read_b128 v[4:7], v75 offset:8208\n"
    "ds_read_b64 v[8:9], v74 offset:0\n"
    "ds_read_b64 v[10:11], v74 offset:2048\n"
    "ds_read_b64 v[12:13], v74 offset:4096\n"
    "ds_read_b64 v[14:15], v74 offset:6144\n"
    "s_waitcnt lgkmcnt(4)\n"
    "v_max_f32 v16, v0, v2\n"
    "v_max3_f32 v16, v16, v4, v6\n"
    "v_sub_f32 v0, v0, v16\n"
    "v_sub_f32 v2, v2, v16\n"
    "v_sub_f32 v4, v4, v16\n"
    "v_sub_f32 v6, v6, v16\n"
    "v_exp_f32 v0, v0\n"
    "v_exp_f32 v2, v2\n"
    "v_exp_f32 v4, v4\n"
    "v_exp_f32 v6, v6\n"
    "v_mul_f32 v17, v0, v1\n"
    "v_fmac_f32 v17, v2, v3\n"
    "v_fmac_f32 v17, v4, v5\n"
    "v_fmac_f32 v17, v6, v7\n"
    "v_rcp_f32 v18, v17\n"
    "v_log_f32 v19, v17\n"
    "s_waitcnt lgkmcnt(0)\n"
    "v_mul_f32 v20, v0, v8\n"
    "v_mul_f32 v21, v0, v9\n"
    "v_fmac_f32 v20, v2, v10\n"
    "v_fmac_f32 v21, v2, v11\n"
    "v_fmac_f32 v20, v4, v12\n"
    "v_fmac_f32 v21, v4, v13\n"
    "v_fmac_f32 v20, v6, v14\n"
    "v_fmac_f32 v21, v6, v15\n"
    "v_mul_f32 v20, v20, v18\n"
    "v_mul_f32 v21, v21, v18\n"
    "v_add_f32 v19, v19, v16\n"
    "v_cvt_pk_bf16_f32 v20, v20, v21\n"
    "v_min_u32 v19, -2, v19\n"
    "v_min_u32 v20, -2, v20\n"
    "buffer_store_dword v20, v76, s[0:3], s44 offen sc1\n"
    "s_mov_b64 exec, 1\n"
    "buffer_store_dword v19, off, s[0:3], s45 sc1\n"
    "s_mov_b64 exec, -1\n"
    "s_memtime s[58:59]\n"
    "s_setpc_b64 s[56:57]\n"
    ".Lwo_%=:\n"
    "v_lshrrev_b32 v62, 16, v83\n"
    "v_xor_b32 v63, 32, v62\n"
    "ds_read_b64_tr_b16 v[0:1], v62 offset:0\n"
    "ds_read_b64_tr_b16 v[2:3], v62 offset:256\n"
    "ds_read_b64_tr_b16 v[4:5], v63 offset:0\n"
    "ds_read_b64_tr_b16 v[6:7], v63 offset:256\n"
    "ds_read_b64_tr_b16 v[8:9], v62 offset:2048\n"
    "ds_read_b64_tr_b16 v[10:11], v62 offset:2304\n"
    "ds_read_b64_tr_b16 v[12:13], v63 offset:2048\n"
    "ds_read_b64_tr_b16 v[14:15], v63 offset:2304\n"
    "ds_read_b64_tr_b16 v[16:17], v62 offset:4096\n"
    "ds_read_b64_tr_b16 v[18:19], v62 offset:4352\n"
    "ds_read_b64_tr_b16 v[20:21], v63 offset:4096\n"
    "ds_read_b64_tr_b16 v[22:23], v63 offset:4352\n"
    "ds_read_b64_tr_b16 v[24:25], v62 offset:6144\n"
    "ds_read_b64_tr_b16 v[26:27], v62 offset:6400\n"
    "ds_read_b64_tr_b16 v[28:29], v63 offset:6144\n"
    "ds_read_b64_tr_b16 v[30:31], v63 offset:6400\n"
    ".Lwo3_%=:\n"
    "v_and_b32 v32, 0xffff, v84\n"
    "v_lshrrev_b32 v33, 16, v84\n"
    "v_and_b32 v64, 0xffff, v87\n"
    "v_and_b32 v65, 0xffff, v85\n"
    "v_lshlrev_b32 v66, 1, v95\n"
    "s_mov_b32 s42, 0\n"
    "s_cmp_lg_u32 s33, 0\n"
    "s_cbranch_scc1 .L127_%=\n"
    "s_waitcnt lgkmcnt(0)\n"
    "s_add_u32 s99, s58, s70\n"
    ".L129_%=:\n"
    "s_memtime s[100:101]\n"
    "s_waitcnt lgkmcnt(0)\n"
    "s_sub_u32 s98, s100, s99\n"
    "s_cmp_lt_i32 s98, 0\n"
    "s_cbranch_scc1 .L129_%=\n"
    ".L128_%=:\n"
    "s_add_u32 s44, s34, s26\n"
    "s_add_u32 s46, s34, s27\n"
    ".L134_%=:\n"
    "buffer_load_dword v60, v33, s[0:3], s46 offen sc1\n"
    "buffer_load_dwordx4 v[44:47], v32, s[0:3], s44 offen offset:0 sc1\n"
    "buffer_load_dwordx4 v[48:51], v32, s[0:3], s44 offen offset:1024 sc1\n"
    "buffer_load_dwordx4 v[52:55], v32, s[0:3], s44 offen offset:2048 sc1\n"
    "buffer_load_dwordx4 v[56:59], v32, s[0:3], s44 offen offset:3072 sc1\n"
    "s_cmp_eq_u32 s55, 0\n"
    "s_cbranch_scc1 .L142_%=\n"
    "s_mov_b32 s55, 0\n"
    "s_sleep 3\n"
    "v_lshrrev_b32 v67, 6, v95\n"
    "v_lshlrev_b32 v67, 9, v67\n"
    "v_and_or_b32 v67, v80, 3, v67\n"
    "v_lshlrev_b32 v67, 4, v67\n"
    "s_add_u32 m0, s54, 0x0\n"
    "s_add_u32 s71, s31, 0x0\n"
    "buffer_load_dwordx4 v67, s[4:7], s71 offen lds\n"
    "s_add_u32 m0, s54, 0x400\n"
    "s_add_u32 s71, s31, 0x20000\n"
    "buffer_load_dwordx4 v67, s[4:7], s71 offen lds\n"
    "s_add_u32 m0, s54, 0x800\n"
    "s_add_u32 s71, s31, 0x40000\n"
    "buffer_load_dwordx4 v67, s[4:7], s71 offen lds\n"
    "s_add_u32 m0, s54, 0xc00\n"
    "s_add_u32 s71, s31, 0x60000\n"
    "buffer_load_dwordx4 v67, s[4:7], s71 offen lds\n"
    "s_add_u32 m0, s54, 0x1000\n"
    "s_add_u32 s71, s31, 0x80000\n"
    "buffer_load_dwordx4 v67, s[4:7], s71 offen lds\n"
    "s_add_u32 m0, s54, 0x1400\n"
    "s_add_u32 s71, s31, 0xa0000\n"
    "buffer_load_dwordx4 v67, s[4:7], s71 offen lds\n"
    "s_add_u32 m0, s54, 0x1800\n"
    "s_add_u32 s71, s31, 0xc0000\n"
    "buffer_load_dwordx4 v67, s[4:7], s71 offen lds\n"
    "s_add_u32 m0, s54, 0x1c00\n"
    "s_add_u32 s71, s31, 0xe0000\n"
    "buffer_load_dwordx4 v67, s[4:7], s71 offen lds\n"
    "s_add_i32 s60, s89, 1\n"
    "s_add_i32 s90, s90, s64\n"
    "s_cmp_lt_i32 s89, s90\n"
    "s_cbranch_scc1 .L140_%=\n"
    "s_waitcnt vmcnt(8)\n"
    ".L141_%=:\n"
    "s_cmp_lg_u32 s42, 0\n"
    "s_cbranch_scc1 .L135_%=\n"
    ".L136_%=:\n"
    "v_mfma_f32_16x16x32_bf16 v[36:39], v[44:47], v[0:3], 0\n"
    "v_max_f32_dpp v67, v60, v60 quad_perm:[1,0,3,2] row_mask:0xf bank_mask:0xf\n"
    "v_mfma_f32_16x16x32_bf16 v[40:43], v[44:47], v[4:7], 0\n"
    "s_nop 0\n"
    "v_max_f32_dpp v67, v67, v67 quad_perm:[2,3,0,1] row_mask:0xf bank_mask:0xf\n"
    "v_mfma_f32_16x16x32_bf16 v[36:39], v[48:51], v[8:11], v[36:39]\n"
    "s_nop 0\n"
    "v_max_f32_dpp v67, v67, v67 row_half_mirror row_mask:0xf bank_mask:0xf\n"
    "v_mfma_f32_16x16x32_bf16 v[40:43], v[48:51], v[12:15], v[40:43]\n"
    "v_sub_f32 v67, v60, v67\n"
    "v_mfma_f32_16x16x32_bf16 v[36:39], v[52:55], v[16:19], v[36:39]\n"
    "v_exp_f32 v67, v67\n"
    "v_mfma_f32_16x16x32_bf16 v[40:43], v[52:55], v[20:23], v[40:43]\n"
    "v_mfma_f32_16x16x32_bf16 v[36:39], v[56:59], v[24:27], v[36:39]\n"
    "v_add_f32_dpp v61, v67, v67 quad_perm:[1,0,3,2] row_mask:0xf bank_mask:0xf\n"
    "v_mfma_f32_16x16x32_bf16 v[40:43], v[56:59], v[28:31], v[40:43]\n"
    "s_nop 0\n"
    "v_add_f32_dpp v61, v61, v61 quad_perm:[2,3,0,1] row_mask:0xf bank_mask:0xf\n"
    "s_nop 1\n"
    "v_add_f32_dpp v61, v61, v61 row_half_mirror row_mask:0xf bank_mask:0xf\n"
    "v_rcp_f32 v61, v61\n"
    "s_nop 0\n"
    "v_mul_f32 v67, v67, v61\n"
    "s_nop 1\n"
    "v_mul_f32_dpp v62, v67, v36 row_newbcast:0 row_mask:0xf bank_mask:0xf\n"
    "v_mul_f32_dpp v63, v67, v40 row_newbcast:0 row_mask:0xf bank_mask:0xf\n"
    "v_fmac_f32_dpp v62, v67, v37 row_newbcast:1 row_mask:0xf bank_mask:0xf\n"
    "v_fmac_f32_dpp v63, v67, v41 row_newbcast:1 row_mask:0xf bank_mask:0xf\n"
    "v_fmac_f32_dpp v62, v67, v38 row_newbcast:2 row_mask:0xf bank_mask:0xf\n"
    "v_fmac_f32_dpp v63, v67, v42 row_newbcast:2 row_mask:0xf bank_mask:0xf\n"
    "v_fmac_f32_dpp v62, v67, v39 row_newbcast:3 row_mask:0xf bank_mask:0xf\n"
    "v_fmac_f32_dpp v63, v67, v43 row_newbcast:3 row_mask:0xf bank_mask:0xf\n"
    "v_cmp_u_f32 vcc, v62, v63\n"
    "s_cbranch_vccnz .L137_%=\n"
    ".L138_%=:\n"
    "ds_write2st64_b32 v64, v62, v63 offset1:2\n"
    "s_cmp_lt_i32 s89, s90\n"
    "s_cbranch_scc1 .L147_%=\n"
    ".L148_%=:\n"
    "s_add_u32 s44, s34, s28\n"
    "s_xor_b32 s45, s44, 0x200000\n"
    "s_cmp_lg_u32 s21, 0\n"
    "s_waitcnt lgkmcnt(0)\n"
    "s_barrier\n"
    "s_cbranch_scc1 .L149_%=\n"
    "ds_read_b128 v[44:47], v66 offset:8320\n"
    "ds_read_b128 v[48:51], v66 offset:8336\n"
    "s_waitcnt lgkmcnt(1)\n"
    "v_add_f32 v44, v44, v45\n"
    "v_add_f32 v46, v46, v47\n"
    "s_waitcnt lgkmcnt(0)\n"
    "v_add_f32 v48, v48, v49\n"
    "v_add_f32 v50, v50, v51\n"
    "v_add_f32 v44, v44, v46\n"
    "v_add_f32 v48, v48, v50\n"
    "v_add_f32 v44, v44, v48\n"
    "s_nop 1\n"
    "v_mov_b32_dpp v45, v44 quad_perm:[1,0,3,2] row_mask:0xf bank_mask:0xf\n"
    "v_cvt_pk_bf16_f32 v44, v44, v45\n"
    "v_min_u32 v44, -2, v44\n"
    "s_mov_b64 exec, s[50:51]\n"
    "buffer_store_dword v44, v65, s[0:3], s44 offen sc1\n"
    "buffer_store_dword v44, v65, s[0:3], s45 offen sc1\n"
    "s_mov_b64 exec, -1\n"
    ".L149_%=:\n"
    "s_memtime s[58:59]\n"
    "s_setpc_b64 s[56:57]\n"
    ".Lrq_%=:\n"
    ".Lrq2_%=:\n"
    "v_mov_b32 v60, 0x3f803f80\n"
    "v_mov_b32 v61, 0x3f803f80\n"
    "v_mov_b32 v62, 0x3f803f80\n"
    "v_mov_b32 v63, 0x3f803f80\n"
    "v_mov_b32 v64, 0x3f803f80\n"
    "v_mov_b32 v65, 0x3f803f80\n"
    "v_mov_b32 v66, 0x3f803f80\n"
    "v_mov_b32 v67, 0x3f803f80\n"
    "v_mov_b32 v68, 0x3f803f80\n"
    "v_mov_b32 v69, 0x3f803f80\n"
    "v_mov_b32 v70, 0x3f803f80\n"
    "v_mov_b32 v71, 0x3f803f80\n"
    "v_mov_b32 v72, 0x3f803f80\n"
    "v_mov_b32 v73, 0x3f803f80\n"
    "v_mov_b32 v74, 0x3f803f80\n"
    "v_mov_b32 v75, 0x3f803f80\n"
    "v_and_b32 v76, 0xffff, v86\n"
    "v_lshrrev_b32 v78, 9, v95\n"
    "v_mov_b32 v48, 0\n"
    "v_mov_b32 v49, 0\n"
    "v_mov_b32 v50, 0\n"
    "v_mov_b32 v51, 0\n"
    "v_mov_b32 v52, 0\n"
    "v_mov_b32 v53, 0\n"
    "v_mov_b32 v54, 0\n"
    "v_mov_b32 v55, 0\n"
    "v_and_b32 v33, 16, v95\n"
    "v_mov_b32 v32, 0x3f80\n"
    "v_lshlrev_b32 v33, v33, v32\n"
    "s_mov_b32 exec_lo, 0x30003\n"
    "s_mov_b32 exec_hi, 0x30003\n"
    "v_mov_b32 v48, v33\n"
    "s_lshl_b64 exec, exec, 2\n"
    "v_mov_b32 v49, v33\n"
    "s_lshl_b64 exec, exec, 2\n"
    "v_mov_b32 v50, v33\n"
    "s_lshl_b64 exec, exec, 2\n"
    "v_mov_b32 v51, v33\n"
    "s_lshl_b64 exec, exec, 2\n"
    "v_mov_b32 v52, v33\n"
    "s_lshl_b64 exec, exec, 2\n"
    "v_mov_b32 v53, v33\n"
    "s_lshl_b64 exec, exec, 2\n"
    "v_mov_b32 v54, v33\n"
    "s_lshl_b64 exec, exec, 2\n"
    "v_mov_b32 v55, v33\n"
    "s_lshl_b64 exec, exec, 2\n"
    "s_mov_b64 exec, -1\n"
    "s_mov_b32 s42, 0\n"
    "s_add_u32 s44, s34, s29\n"
    "s_waitcnt lgkmcnt(0)\n"
    "s_add_u32 s99, s58, s70\n"
    ".L151_%=:\n"
    "s_memtime s[100:101]\n"
    "s_waitcnt lgkmcnt(0)\n"
    "s_sub_u32 s98, s100, s99\n"
    "s_cmp_lt_i32 s98, 0\n"
    "s_cbranch_scc1 .L151_%=\n"
    "buffer_load_dwordx4 v[32:35], v95, s[0:3], s44 offen offset:0 sc1\n"
    "buffer_load_dwordx4 v[36:39], v95, s[0:3], s44 offen offset:1024 sc1\n"
    "buffer_load_dwordx4 v[40:43], v95, s[0:3], s44 offen offset:2048 sc1\n"
    "buffer_load_dwordx4 v[44:47], v95, s[0:3], s44 offen offset:3072 sc1\n"
    "v_lshrrev_b32 v56, 16, v85\n"
    "s_waitcnt vmcnt(4)\n"
    "v_xor_b32 v57, 32, v56\n"
    "ds_read_b64_tr_b16 v[0:1], v56 offset:0\n"
    "ds_read_b64_tr_b16 v[2:3], v56 offset:512\n"
    "ds_read_b64_tr_b16 v[4:5], v57 offset:0\n"
    "ds_read_b64_tr_b16 v[6:7], v57 offset:512\n"
    "ds_read_b64_tr_b16 v[8:9], v56 offset:1024\n"
    "ds_read_b64_tr_b16 v[10:11], v56 offset:1536\n"
    "ds_read_b64_tr_b16 v[12:13], v57 offset:1024\n"
    "ds_read_b64_tr_b16 v[14:15], v57 offset:1536\n"
    "ds_read_b64_tr_b16 v[16:17], v56 offset:2048\n"
    "ds_read_b64_tr_b16 v[18:19], v56 offset:2560\n"
    "ds_read_b64_tr_b16 v[20:21], v57 offset:2048\n"
    "ds_read_b64_tr_b16 v[22:23], v57 offset:2560\n"
    "ds_read_b64_tr_b16 v[24:25], v56 offset:3072\n"
    "ds_read_b64_tr_b16 v[26:27], v56 offset:3584\n"
    "ds_read_b64_tr_b16 v[28:29], v57 offset:3072\n"
    "ds_read_b64_tr_b16 v[30:31], v57 offset:3584\n"
    "s_waitcnt lgkmcnt(0)\n"
    ".L157_%=:\n"
    "v_lshlrev_b32 v56, 16, v88\n"
    "v_and_b32 v57, 0xffff0000, v88\n"
    "v_lshlrev_b32 v58, 16, v89\n"
    "v_and_b32 v59, 0xffff0000, v89\n"
    "s_cmp_eq_u32 s55, 0\n"
    "s_cbranch_scc1 .L161_%=\n"
    "s_mov_b32 s55, 0\n"
    "v_lshrrev_b32 v79, 8, v80\n"
    "v_lshlrev_b32 v79, 10, v79\n"
    "v_and_or_b32 v79, v80, 48, v79\n"
    "s_add_u32 m0, s54, 0x0\n"
    "s_add_u32 s71, s32, 0x0\n"
    "buffer_load_dwordx4 v79, s[4:7], s71 offen lds\n"
    "s_add_u32 m0, s54, 0x400\n"
    "s_add_u32 s71, s32, 0x1000\n"
    "buffer_load_dwordx4 v79, s[4:7], s71 offen lds\n"
    "s_add_u32 m0, s54, 0x800\n"
    "s_add_u32 s71, s32, 0x8000\n"
    "buffer_load_dwordx4 v79, s[4:7], s71 offen lds\n"
    "s_add_u32 m0, s54, 0xc00\n"
    "s_add_u32 s71, s32, 0x9000\n"
    "buffer_load_dwordx4 v79, s[4:7], s71 offen lds\n"
    "s_add_u32 m0, s54, 0x1000\n"
    "s_add_u32 s71, s32, 0x10000\n"
    "buffer_load_dwordx4 v79, s[4:7], s71 offen lds\n"
    "s_add_u32 m0, s54, 0x1400\n"
    "s_add_u32 s71, s32, 0x11000\n"
    "buffer_load_dwordx4 v79, s[4:7], s71 offen lds\n"
    "s_add_u32 m0, s54, 0x1800\n"
    "s_add_u32 s71, s32, 0x18000\n"
    "buffer_load_dwordx4 v79, s[4:7], s71 offen lds\n"
    "s_add_u32 m0, s54, 0x1c00\n"
    "s_add_u32 s71, s32, 0x19000\n"
    "buffer_load_dwordx4 v79, s[4:7], s71 offen lds\n"
    "s_add_i32 s60, s89, 1\n"
    "s_add_i32 s90, s90, s64\n"
    "s_cmp_lt_i32 s89, s90\n"
    "s_cbranch_scc1 .L159_%=\n"
    "s_waitcnt vmcnt(8)\n"
    ".L160_%=:\n"
    "s_cmp_lg_u32 s42, 0\n"
    "s_cbranch_scc1 .L153_%=\n"
    ".L154_%=:\n"
    "v_mfma_f32_16x16x32_bf16 v[56:59], v[48:51], v[32:35], v[56:59]\n"
    "v_mfma_f32_16x16x32_bf16 v[56:59], v[52:55], v[36:39], v[56:59]\n"
    "v_mfma_f32_16x16x32_bf16 v[56:59], v[48:51], v[40:43], v[56:59]\n"
    "v_mfma_f32_16x16x32_bf16 v[56:59], v[52:55], v[44:47], v[56:59]\n"
    "s_nop 7\n"
    "v_cmp_u_f32 vcc, v56, v56\n"
    "s_cbranch_vccnz .L155_%=\n"
    ".L156_%=:\n"
    "v_cvt_pk_bf16_f32 v88, v56, v57\n"
    "v_cvt_pk_bf16_f32 v89, v58, v59\n"
    "s_cmp_lg_u32 s33, 0\n"
    "s_cbranch_scc1 .L166_%=\n"
    ".Lwq_%=:\n"
    "v_mov_b32_dpp v60, v88 quad_perm:[0,1,2,3] row_mask:0xf bank_mask:0x1\n"
    "v_mov_b32_dpp v61, v89 quad_perm:[0,1,2,3] row_mask:0xf bank_mask:0x1\n"
    "v_mov_b32_dpp v62, v88 row_shl:2 row_mask:0xf bank_mask:0x1\n"
    "v_mov_b32_dpp v63, v89 row_shl:2 row_mask:0xf bank_mask:0x1\n"
    "v_mov_b32_dpp v64, v88 row_shl:4 row_mask:0xf bank_mask:0x1\n"
    "v_mov_b32_dpp v65, v89 row_shl:4 row_mask:0xf bank_mask:0x1\n"
    "v_mfma_f32_16x16x32_bf16 v[40:43], v[60:63], v[60:63], 0\n"
    "v_mov_b32_dpp v66, v88 row_shl:6 row_mask:0xf bank_mask:0x1\n"
    "v_mov_b32_dpp v67, v89 row_shl:6 row_mask:0xf bank_mask:0x1\n"
    "v_mfma_f32_16x16x32_bf16 v[32:35], v[60:63], v[0:3], 0\n"
    "v_mov_b32_dpp v68, v88 row_shl:8 row_mask:0xf bank_mask:0x1\n"
    "v_mov_b32_dpp v69, v89 row_shl:8 row_mask:0xf bank_mask:0x1\n"
    "v_mfma_f32_16x16x32_bf16 v[36:39], v[60:63], v[4:7], 0\n"
    "v_mov_b32_dpp v70, v88 row_shl:10 row_mask:0xf bank_mask:0x1\n"
    "v_mov_b32_dpp v71, v89 row_shl:10 row_mask:0xf bank_mask:0x1\n"
    "v_mfma_f32_16x16x32_bf16 v[40:43], v[64:67], v[64:67], v[40:43]\n"
    "v_mov_b32_dpp v72, v88 row_shl:12 row_mask:0xf bank_mask:0x1\n"
    "v_mov_b32_dpp v73, v89 row_shl:12 row_mask:0xf bank_mask:0x1\n"
    "v_mfma_f32_16x16x32_bf16 v[32:35], v[64:67], v[8:11], v[32:35]\n"
    "v_mov_b32_dpp v74, v88 row_shl:14 row_mask:0xf bank_mask:0x1\n"
    "v_mov_b32_dpp v75, v89 row_shl:14 row_mask:0xf bank_mask:0x1\n"
    "v_mfma_f32_16x16x32_bf16 v[36:39], v[64:67], v[12:15], v[36:39]\n"
    "v_mul_u32_u24 v79, 0x110, v78\n"
    "v_lshlrev_b32 v78, 8, v78\n"
    "v_mfma_f32_16x16x32_bf16 v[40:43], v[68:71], v[68:71], v[40:43]\n"
    "v_lshrrev_b32 v46, 16, v87\n"
    "v_and_b32 v44, 0x4f0, v46\n"
    "v_mfma_f32_16x16x32_bf16 v[32:35], v[68:71], v[16:19], v[32:35]\n"
    "v_lshrrev_b32 v45, 3, v95\n"
    "v_mfma_f32_16x16x32_bf16 v[36:39], v[68:71], v[20:23], v[36:39]\n"
    "v_mfma_f32_16x16x32_bf16 v[40:43], v[72:75], v[72:75], v[40:43]\n"
    "v_mfma_f32_16x16x32_bf16 v[32:35], v[72:75], v[24:27], v[32:35]\n"
    "v_mfma_f32_16x16x32_bf16 v[36:39], v[72:75], v[28:31], v[36:39]\n"
    "s_mov_b32 exec_hi, 0\n"
    "s_add_u32 s44, s34, 0x34000\n"
    "s_add_u32 s44, s44, s30\n"
    "s_xor_b32 s45, s44, 0x200000\n"
    "s_nop 1\n"
    "ds_write2st64_b32 v76, v40, v41 offset0:8 offset1:9\n"
    "ds_write2st64_b32 v76, v32, v33 offset0:0 offset1:1\n"
    "ds_write2st64_b32 v76, v36, v37 offset0:4 offset1:5\n"
    "s_mov_b64 exec, -1\n"
    "s_cmp_lt_i32 s89, s90\n"
    "s_cbranch_scc1 .L168_%=\n"
    ".L169_%=:\n"
    "s_cmp_lg_u32 s21, 0\n"
    "s_waitcnt lgkmcnt(0)\n"
    "s_barrier\n"
    "s_cbranch_scc1 .L167_%=\n"
    "ds_read_b128 v[32:35], v78 offset:12480\n"
    "ds_read_b128 v[36:39], v79 offset:12416\n"
    "ds_read_b128 v[40:43], v44 offset:10880\n"
    "ds_read_b128 v[48:51], v46 offset:10368\n"
    "s_waitcnt lgkmcnt(3)\n"
    "v_add_f32 v32, v32, v33\n"
    "v_add_f32 v34, v34, v35\n"
    "v_add_f32 v32, v32, v34\n"
    "s_waitcnt lgkmcnt(2)\n"
    "v_add_f32 v36, v36, v37\n"
    "v_add_f32 v38, v38, v39\n"
    "v_add_f32 v36, v36, v38\n"
    "v_mul_f32 v32, 0x3b000000, v32\n"
    "v_mul_f32 v36, 0x3b000000, v36\n"
    "v_fma_f32 v36, -v32, v32, v36\n"
    "v_max_f32 v36, 0, v36\n"
    "v_add_f32 v36, 0x3727c5ac, v36\n"
    "v_rsq_f32 v36, v36\n"
    "s_waitcnt lgkmcnt(1)\n"
    "v_add_f32 v40, v40, v41\n"
    "v_add_f32 v42, v42, v43\n"
    "v_add_f32 v40, v40, v42\n"
    "s_waitcnt lgkmcnt(0)\n"
    "v_add_f32 v48, v48, v49\n"
    "v_add_f32 v50, v50, v51\n"
    "v_add_f32 v48, v48, v50\n"
    "v_fma_f32 v48, -v32, v40, v48\n"
    "v_mul_f32 v48, v48, v36\n"
    "s_nop 1\n"
    "v_mov_b32_dpp v49, v48 quad_perm:[1,0,3,2] row_mask:0xf bank_mask:0xf\n"
    "v_cvt_pk_bf16_f32 v48, v48, v49\n"
    "v_min_u32 v48, -2, v48\n"
    "s_mov_b64 exec, s[50:51]\n"
    "buffer_store_dword v48, v45, s[0:3], s44 offen sc1\n"
    "buffer_store_dword v48, v45, s[0:3], s45 offen sc1\n"
    "s_mov_b64 exec, -1\n"
    ".L167_%=:\n"
    "s_memtime s[58:59]\n"
    "s_setpc_b64 s[56:57]\n"
    ".Latt1_%=:\n"
    "v_and_b32 v18, 0xffff, v81\n"
    "s_add_u32 s45, s34, s22\n"
    "s_lshl_b32 s44, s35, 13\n"
    "s_add_u32 s44, s44, s92\n"
    "v_add_u32 v37, s44, v95\n"
    "s_cmp_eq_u32 s55, 2\n"
    "s_cbranch_scc1 .L171_%=\n"
    "s_add_i32 s44, s36, -24\n"
    "s_cmp_lt_i32 s91, s44\n"
    "s_cbranch_scc1 .L173_%=\n"
    ".L174_%=:\n"
    "buffer_load_dwordx4 v[0:3], v18, s[0:3], s45 offen sc1\n"
    "buffer_load_dwordx4 v[4:7], v18, s[0:3], s45 offen offset:256 sc1\n"
    "s_cmp_lt_i32 s89, s90\n"
    "s_cbranch_scc1 .L176_%=\n"
    "s_waitcnt vmcnt(0)\n"
    "s_mov_b32 s91, s89\n"
    ".L177_%=:\n"
    ".L171_%=:\n"
    "s_add_i32 s44, s36, -16\n"
    "s_cmp_lt_i32 s91, s44\n"
    "s_cbranch_scc1 .L180_%=\n"
    ".L181_%=:\n"
    "s_branch .Latt2_%=\n"
    ".Lown_%=:\n"
    "v_mov_b32 v36, -1\n"
    "v_mov_b32 v37, -1\n"
    "v_mov_b32 v38, -1\n"
    "v_mov_b32 v39, -1\n"
    "v_lshrrev_b32 v33, 8, v95\n"
    "v_mul_u32_u24 v33, 0x34000, v33\n"
    "v_and_b32 v34, 0xc0, v95\n"
    "v_lshl_add_u32 v33, v34, 4, v33\n"
    "v_and_or_b32 v33, v95, 48, v33\n"
    "v_lshrrev_b32 v34, 7, v95\n"
    "v_mul_u32_u24 v34, 0x34000, v34\n"
    "v_and_b32 v35, 0x70, v95\n"
    "v_add_u32 v34, v34, v35\n"
    "v_lshrrev_b32 v35, 8, v95\n"
    "v_mul_u32_u24 v35, 0x34000, v35\n"
    "s_mov_b32 vcc_lo, 0xffff0000\n"
    "s_mov_b32 vcc_hi, 0xffff0000\n"
    "s_andn2_b32 s44, s23, 0x100\n"
    "s_add_u32 s44, s44, s43\n"
    "s_andn2_b32 s45, s24, 0x100\n"
    "s_add_u32 s45, s45, s43\n"
    "s_lshr_b32 s98, s63, 5\n"
    "s_lshr_b32 s99, s62, 5\n"
    "v_mov_b32 v40, s98\n"
    "v_mov_b32 v41, s99\n"
    "v_cndmask_b32 v40, v40, v41, vcc\n"
    "v_add_u32 v41, v40, v35\n"
    "v_add_u32 v40, v40, v33\n"
    "s_mov_b32 exec_lo, s70\n"
    "buffer_store_dwordx4 v[36:39], v40, s[0:3], s44 offen sc1\n"
    "s_and_b32 exec_lo, exec_lo, 0x10001\n"
    "s_mov_b32 exec_hi, 0x10001\n"
    "buffer_store_dword v36, v41, s[0:3], s45 offen sc1\n"
    "s_mov_b64 exec, -1\n"
    "s_add_u32 s44, s44, 0xd0000\n"
    "s_add_u32 s45, s45, 0xd0000\n"
    "s_lshr_b32 s98, s61, 5\n"
    "s_lshr_b32 s99, s62, 5\n"
    "v_mov_b32 v40, s98\n"
    "v_mov_b32 v41, s99\n"
    "v_cndmask_b32 v40, v40, v41, vcc\n"
    "v_add_u32 v41, v40, v35\n"
    "v_add_u32 v40, v40, v33\n"
    "buffer_store_dwordx4 v[36:39], v40, s[0:3], s44 offen sc1\n"
    "s_and_b32 exec_lo, exec_lo, 0x10001\n"
    "s_mov_b32 exec_hi, 0x10001\n"
    "buffer_store_dword v36, v41, s[0:3], s45 offen sc1\n"
    "s_mov_b64 exec, -1\n"
    "s_add_u32 s44, s44, 0xd0000\n"
    "s_add_u32 s45, s45, 0xd0000\n"
    "s_cmp_lt_u32 s21, 2\n"
    "s_cselect_b32 s44, s28, s30\n"
    "s_cselect_b32 exec_lo, -1, 0xffffff00\n"
    "s_mov_b32 exec_hi, exec_lo\n"
    "s_and_b32 s45, s21, 1\n"
    "s_lshl_b32 s45, s45, 21\n"
    "s_xor_b32 s44, s44, s45\n"
    "s_add_u32 s44, s44, s43\n"
    "buffer_store_dwordx4 v[36:39], v34, s[0:3], s44 offen sc1\n"
    "s_mov_b64 exec, -1\n"
    "s_setpc_b64 s[56:57]\n"
    ".L4_%=:\n"
    "s_cmp_lt_i32 s89, s90\n"
    "s_cbranch_scc0 .L6_%=\n"
    "s_waitcnt vmcnt(8)\n"
    "s_sub_i32 s98, s89, 4\n"
    "s_max_i32 s91, s91, s98\n"
    "s_swappc_b64 s[96:97], s[94:95]\n"
    "s_cmp_lt_i32 s91, 7\n"
    "s_cbranch_scc1 .L4_%=\n"
    "s_branch .L5_%=\n"
    ".L6_%=:\n"
    "s_waitcnt vmcnt(8)\n"
    "s_sub_i32 s98, s89, 4\n"
    "s_max_i32 s91, s91, s98\n"
    "s_cmp_ge_i32 s91, 7\n"
    "s_cbranch_scc1 .L5_%=\n"
    "s_waitcnt vmcnt(0)\n"
    "s_mov_b32 s91, s89\n"
    "s_branch .L5_%=\n"
    ".L7_%=:\n"
    "s_cmp_lt_i32 s89, s90\n"
    "s_cbranch_scc0 .L9_%=\n"
    "s_waitcnt vmcnt(8)\n"
    "s_sub_i32 s98, s89, 4\n"
    "s_max_i32 s91, s91, s98\n"
    "s_swappc_b64 s[96:97], s[94:95]\n"
    "s_cmp_lt_i32 s91, 7\n"
    "s_cbranch_scc1 .L7_%=\n"
    "s_branch .L8_%=\n"
    ".L9_%=:\n"
    "s_waitcnt vmcnt(8)\n"
    "s_sub_i32 s98, s89, 4\n"
    "s_max_i32 s91, s91, s98\n"
    "s_cmp_ge_i32 s91, 7\n"
    "s_cbranch_scc1 .L8_%=\n"
    "s_waitcnt vmcnt(0)\n"
    "s_mov_b32 s91, s89\n"
    "s_branch .L8_%=\n"
    ".L12_%=:\n"
    "s_add_i32 s98, s89, 5\n"
    "s_cmp_le_i32 s98, s90\n"
    "s_cbranch_scc0 .L14_%=\n"
    "s_waitcnt vmcnt(11)\n"
    "s_swappc_b64 s[96:97], s[94:95]\n"
    "s_waitcnt vmcnt(11)\n"
    "s_swappc_b64 s[96:97], s[94:95]\n"
    "s_waitcnt vmcnt(11)\n"
    "s_swappc_b64 s[96:97], s[94:95]\n"
    "s_waitcnt vmcnt(11)\n"
    "s_swappc_b64 s[96:97], s[94:95]\n"
    "s_waitcnt vmcnt(11)\n"
    "s_swappc_b64 s[96:97], s[94:95]\n"
    "s_waitcnt vmcnt(10)\n"
    "s_sub_i32 s98, s89, 5\n"
    "s_max_i32 s91, s91, s98\n"
    "s_branch .L13_%=\n"
    ".L14_%=:\n"
    "s_mov_b32 s99, s89\n"
    ".L15_%=:\n"
    "s_swappc_b64 s[96:97], s[94:95]\n"
    "s_cmp_lt_i32 s89, s90\n"
    "s_cbranch_scc1 .L15_%=\n"
    "s_max_i32 s91, s91, s99\n"
    "s_sub_i32 s98, s89, s99\n"
    "s_waitcnt vmcnt(8)\n"
    "s_cmp_ge_i32 s98, 4\n"
    "s_cbranch_scc1 .L13_%=\n"
    "s_waitcnt vmcnt(6)\n"
    "s_cmp_ge_i32 s98, 3\n"
    "s_cbranch_scc1 .L13_%=\n"
    "s_waitcnt vmcnt(4)\n"
    "s_cmp_ge_i32 s98, 2\n"
    "s_cbranch_scc1 .L13_%=\n"
    "s_waitcnt vmcnt(2)\n"
    "s_branch .L13_%=\n"
    ".L2_%=:\n"
    "s_waitcnt lgkmcnt(0)\n"
    "s_mov_b32 s47, s58\n"
    "s_mov_b32 s44, 0x804000\n"
    "s_mov_b64 exec, 1\n"
    "buffer_load_dwordx2 v[212:213], off, s[0:3], s44 sc1\n"
    "s_mov_b64 exec, -1\n"
    "s_mov_b64 s[4:5], s[8:9]\n"
    "s_mov_b32 s6, 0x2000\n"
    "v_lshrrev_b32 v0, 16, v86\n"
    "s_movk_i32 s44, 0x1000\n"
    "s_movk_i32 s45, 0x800\n"
    "s_movk_i32 s46, 0x1800\n"
    "buffer_load_dwordx2 v[88:89], v0, s[4:7], 0 offen\n"
    "buffer_load_dwordx2 v[208:209], v0, s[4:7], s44 offen\n"
    "buffer_load_dwordx2 v[210:211], v0, s[4:7], s45 offen\n"
    "buffer_load_dwordx2 v[90:91], v0, s[4:7], s46 offen\n"
    "v_mov_b32 v4, -1\n"
    "v_mov_b32 v5, -1\n"
    "v_mov_b32 v6, -1\n"
    "v_mov_b32 v7, -1\n"
    "s_movk_i32 s98, 0x70\n"
    "v_bfe_u32 v2, v95, 7, 2\n"
    "v_lshlrev_b32 v2, 21, v2\n"
    "v_and_or_b32 v2, v95, s98, v2\n"
    "s_cmp_eq_u32 s21, 1\n"
    "s_cbranch_scc1 .L22_%=\n"
    "s_cmp_eq_u32 s21, 2\n"
    "s_cbranch_scc1 .L23_%=\n"
    "s_cmp_eq_u32 s21, 3\n"
    "s_cbranch_scc1 .L24_%=\n"
    "v_and_b32 v1, 0x3c0, v95\n"
    "v_lshlrev_b32 v1, 4, v1\n"
    "v_and_or_b32 v1, v95, 48, v1\n"
    "s_add_u32 s44, s23, 0x200000\n"
    "buffer_store_dwordx4 v[4:7], v1, s[0:3], s23 offen sc1\n"
    "buffer_store_dwordx4 v[4:7], v1, s[0:3], s44 offen sc1\n"
    "v_bfe_u32 v9, v95, 6, 1\n"
    "v_lshlrev_b32 v9, 21, v9\n"
    "v_and_b32 v3, 48, v95\n"
    "v_lshl_add_u32 v9, v3, 6, v9\n"
    "s_mov_b64 exec, 0xff\n"
    "buffer_store_dword v4, v9, s[0:3], s24 offen offset:0 sc1\n"
    "s_mov_b64 exec, 0xffff\n"
    "buffer_store_dwordx4 v[4:7], v2, s[0:3], s28 offen sc1\n"
    "s_branch .L21_%=\n"
    ".L22_%=:\n"
    "v_lshrrev_b32 v3, 9, v95\n"
    "v_mul_u32_u24 v3, 0xd0000, v3\n"
    "v_add_u32 v8, v2, v3\n"
    "buffer_store_dwordx4 v[4:7], v8, s[0:3], s30 offen sc1\n"
    "s_cmp_lt_u32 s20, 136\n"
    "s_cbranch_scc0 .L21_%=\n"
    "v_bfe_u32 v3, v95, 4, 3\n"
    "v_mul_u32_u24 v3, 0x34000, v3\n"
    "v_bfe_u32 v9, v95, 7, 1\n"
    "v_lshl_add_u32 v3, v9, 22, v3\n"
    "v_lshrrev_b32 v9, 8, v95\n"
    "v_lshl_add_u32 v3, v9, 7, v3\n"
    "s_sub_u32 s44, s25, 128\n"
    "buffer_store_dword v4, v3, s[0:3], s44 offen offset:0 sc1\n"
    "s_cmp_eq_u32 s20, 128\n"
    "s_cbranch_scc0 .L21_%=\n"
    "s_mov_b32 s44, 0x2d000\n"
    "s_mov_b32 exec_hi, 0\n"
    "buffer_store_dword v4, v3, s[0:3], s44 offen offset:0 sc1\n"
    ".L21_%=:\n"
    "s_mov_b64 exec, -1\n"
    "s_branch .L24_%=\n"
    ".L23_%=:\n"
    "s_mov_b64 s[4:5], s[12:13]\n"
    "s_mov_b32 s6, 0x400000\n"
    "s_and_b32 s44, s20, 7\n"
    "s_bfe_u32 s45, s20, 0x40003\n"
    "s_lshl_b32 s44, s44, 10\n"
    "s_lshl_b32 s45, s45, 18\n"
    "s_add_u32 s44, s44, s45\n"
    "v_lshrrev_b32 v8, 7, v95\n"
    "v_lshlrev_b32 v8, 13, v8\n"
    "v_and_b32 v9, 0x70, v95\n"
    "v_lshl_or_b32 v8, v9, 3, v8\n"
    "buffer_load_dword v58, v8, s[4:7], s44 offen\n"
    "s_add_u32 s44, s44, 0x10000\n"
    "buffer_load_dword v58, v8, s[4:7], s44 offen\n"
    "s_add_u32 s44, s44, 0x10000\n"
    "buffer_load_dword v58, v8, s[4:7], s44 offen\n"
    "s_add_u32 s44, s44, 0x10000\n"
    "buffer_load_dword v58, v8, s[4:7], s44 offen\n"
    "s_add_u32 s44, s44, 0x10000\n"
    "s_waitcnt vmcnt(4)\n"
    "s_branch .L24_%=x\n"
    ".L24_%=:\n"
    "s_cmp_lg_u32 s21, 3\n"
    "s_cbranch_scc1 .L25_%=\n"
    "s_mov_b64 s[4:5], s[72:73]\n"
    "s_bfe_u32 s44, s20, 0x20003\n"
    "s_cmp_eq_u32 s44, 1\n"
    "s_cselect_b64 s[4:5], s[76:77], s[4:5]\n"
    "s_cmp_eq_u32 s44, 2\n"
    "s_cselect_b64 s[4:5], s[80:81], s[4:5]\n"
    "s_cmp_eq_u32 s44, 3\n"
    "s_cselect_b64 s[4:5], s[84:85], s[4:5]\n"
    "s_lshl_b32 s46, s47, 16\n"
    "s_and_b32 s46, s46, 0x3c00000\n"
    "s_bitcmp1_b32 s20, 0\n"
    "s_cselect_b64 s[4:5], s[0:1], s[4:5]\n"
    "s_cselect_b32 s46, 0, s46\n"
    "s_add_u32 s44, s46, 0x100000\n"
    "s_load_dword s98, s[4:5], s44 offset:0xbff80\n"
    "s_lshr_b32 s45, s47, 3\n"
    "s_and_b32 s45, s45, 0x780\n"
    "s_or_b32 s46, s46, s45\n"
    "s_lshl_b32 s45, s20, 10\n"
    "s_and_b32 s45, s45, 0x1800\n"
    "s_or_b32 s46, s46, s45\n"
    "s_bfe_u32 s45, s20, 0x40003\n"
    "s_lshl_b32 s45, s45, 14\n"
    "s_or_b32 s46, s46, s45\n"
    "s_add_u32 s46, s46, 0x1c0000\n"
    "s_waitcnt lgkmcnt(0)\n"
    "s_memtime s[100:101]\n"
    "s_waitcnt lgkmcnt(0)\n"
    "s_mov_b32 s99, s100\n"
    "s_load_dword s98, s[4:5], s46\n"
    "s_waitcnt lgkmcnt(0)\n"
    "s_memtime s[100:101]\n"
    "s_waitcnt lgkmcnt(0)\n"
    "s_sub_u32 s44, s100, s99\n"
    "s_min_u32 s44, s44, 0x3fff\n"
    "s_xor_b32 s46, s46, 0x2000\n"
    "s_memtime s[100:101]\n"
    "s_waitcnt lgkmcnt(0)\n"
    "s_mov_b32 s99, s100\n"
    "s_load_dword s98, s[4:5], s46\n"
    "s_waitcnt lgkmcnt(0)\n"
    "s_memtime s[100:101]\n"
    "s_waitcnt lgkmcnt(0)\n"
    "s_sub_u32 s45, s100, s99\n"
    "s_min_u32 s45, s45, 0x3fff\n"
    "s_xor_b32 s46, s46, 0x2000\n"
    "s_min_u32 s98, s44, s45\n"
    "s_cmp_lt_u32 s98, 330\n"
    "s_cselect_b32 s44, 0x3fff, s44\n"
    "s_cselect_b32 s45, 0x3fff, s45\n"
    "s_getreg_b32 s99, hwreg(20, 0, 3)\n"
    "s_bitcmp1_b32 s99, 2\n"
    "s_cselect_b32 s98, s45, s44\n"
    "s_cselect_b32 s45, s44, s45\n"
    "s_lshr_b32 s44, s98, 2\n"
    "s_lshr_b32 s45, s45, 2\n"
    "s_lshl_b32 s45, s45, 12\n"
    "s_or_b32 s44, s44, s45\n"
    "s_lshl_b32 s99, s99, 24\n"
    "s_or_b32 s33, s44, s99\n"
    "s_mov_b32 s35, s46\n"
    ".L25_%=:\n"
    "s_waitcnt vmcnt(0)\n"
    ".L24_%=x:\n"
    "v_readfirstlane_b32 s69, v212\n"
    "v_readfirstlane_b32 s37, v213\n"
    "s_add_u32 s69, s69, 1\n"
    "s_cmp_lg_u32 s21, 3\n"
    "s_cbranch_scc1 .L26_%=\n"
    "s_bfe_u32 s44, s20, 0x40003\n"
    "s_bfe_u32 s45, s20, 0x20001\n"
    "s_lshl_b32 s45, s45, 4\n"
    "s_add_u32 s44, s44, s45\n"
    "s_and_b32 s45, s20, 1\n"
    "s_lshl_b32 s45, s45, 6\n"
    "s_add_u32 s44, s44, s45\n"
    "s_lshl_b32 s44, s44, 3\n"
    "s_add_u32 s44, s44, 0x804080\n"
    "v_mov_b32 v10, s69\n"
    "v_mov_b32 v11, s33\n"
    "s_mov_b64 exec, 1\n"
    "buffer_store_dwordx2 v[10:11], off, s[0:3], s44 sc1\n"
    "s_bitcmp1_b32 s20, 0\n"
    "s_cbranch_scc0 .L27_%=\n"
    "s_xor_b32 s45, s35, 0x2000\n"
    "buffer_store_dword v4, off, s[0:3], s35 sc1\n"
    "buffer_store_dword v4, off, s[0:3], s45 sc1\n"
    ".L27_%=:\n"
    "s_mov_b64 exec, -1\n"
    ".L26_%=:\n"
    "s_barrier\n"
    "s_cmp_lg_u32 s21, 1\n"
    "s_cbranch_scc1 .L18_%=\n"
    "s_and_b32 s44, s20, 3\n"
    "s_lshl_b32 s44, s44, 5\n"
    "s_bfe_u32 s45, s20, 0x10002\n"
    "s_lshl_b32 s45, s45, 4\n"
    "s_add_u32 s44, s44, s45\n"
    "s_bfe_u32 s45, s20, 0x40003\n"
    "s_add_u32 s44, s44, s45\n"
    "s_lshl_b32 s44, s44, 7\n"
    "s_add_u32 s44, s44, 0x800000\n"
    "v_mov_b32 v8, s69\n"
    "v_mov_b32 v9, s37\n"
    "s_mov_b64 exec, 1\n"
    "buffer_store_dwordx2 v[8:9], off, s[0:3], s44 sc1\n"
    "s_mov_b64 exec, -1\n"
    ".L18_%=:\n"
    "s_movk_i32 s70, 5600\n"
    "s_waitcnt lgkmcnt(0)\n"
    "s_add_u32 s99, s58, s70\n"
    ".L28_%=:\n"
    "s_memtime s[100:101]\n"
    "s_waitcnt lgkmcnt(0)\n"
    "s_sub_u32 s98, s100, s99\n"
    "s_cmp_lt_i32 s98, 0\n"
    "s_cbranch_scc1 .L28_%=\n"
    "s_mov_b64 s[4:5], s[12:13]\n"
    "s_mov_b32 s6, 0x400000\n"
    "v_lshrrev_b32 v0, 6, v95\n"
    "v_lshlrev_b32 v0, 9, v0\n"
    "v_and_or_b32 v0, v80, 3, v0\n"
    "v_lshlrev_b32 v0, 4, v0\n"
    "s_add_u32 m0, s54, 0x0\n"
    "s_add_u32 s71, s31, 0x0\n"
    "buffer_load_dwordx4 v0, s[4:7], s71 offen lds\n"
    "s_add_u32 m0, s54, 0x400\n"
    "s_add_u32 s71, s31, 0x20000\n"
    "buffer_load_dwordx4 v0, s[4:7], s71 offen lds\n"
    "s_add_u32 m0, s54, 0x800\n"
    "s_add_u32 s71, s31, 0x40000\n"
    "buffer_load_dwordx4 v0, s[4:7], s71 offen lds\n"
    "s_add_u32 m0, s54, 0xc00\n"
    "s_add_u32 s71, s31, 0x60000\n"
    "buffer_load_dwordx4 v0, s[4:7], s71 offen lds\n"
    "s_add_u32 m0, s54, 0x1000\n"
    "s_add_u32 s71, s31, 0x80000\n"
    "buffer_load_dwordx4 v0, s[4:7], s71 offen lds\n"
    "s_add_u32 m0, s54, 0x1400\n"
    "s_add_u32 s71, s31, 0xa0000\n"
    "buffer_load_dwordx4 v0, s[4:7], s71 offen lds\n"
    "s_add_u32 m0, s54, 0x1800\n"
    "s_add_u32 s71, s31, 0xc0000\n"
    "buffer_load_dwordx4 v0, s[4:7], s71 offen lds\n"
    "s_add_u32 m0, s54, 0x1c00\n"
    "s_add_u32 s71, s31, 0xe0000\n"
    "buffer_load_dwordx4 v0, s[4:7], s71 offen lds\n"
    "s_waitcnt vmcnt(0)\n"
    "s_mov_b64 s[4:5], s[16:17]\n"
    "s_mov_b32 s6, 0x400000\n"
    "s_and_b32 s44, s20, 7\n"
    "s_bfe_u32 s45, s20, 0x40003\n"
    "s_lshl_b32 s45, s45, 2\n"
    "s_add_u32 s45, s45, s21\n"
    "s_lshl_b32 s44, s44, 19\n"
    "s_lshl_b32 s45, s45, 13\n"
    "v_lshlrev_b32 v54, 3, v95\n"
    "s_add_u32 s44, s44, s45\n"
    "s_mov_b32 exec_hi, 0\n"
    "buffer_load_dword v58, v54, s[4:7], s44 offen\n"
    "s_mov_b64 exec, -1\n"
    "s_nop 1\n"
    "v_lshrrev_b32 v78, 16, v85\n"
    "v_xor_b32 v79, 32, v78\n"
    "ds_read_b64_tr_b16 v[0:1], v78 offset:0\n"
    "ds_read_b64_tr_b16 v[2:3], v78 offset:512\n"
    "ds_read_b64_tr_b16 v[4:5], v79 offset:0\n"
    "ds_read_b64_tr_b16 v[6:7], v79 offset:512\n"
    "ds_read_b64_tr_b16 v[8:9], v78 offset:1024\n"
    "ds_read_b64_tr_b16 v[10:11], v78 offset:1536\n"
    "ds_read_b64_tr_b16 v[12:13], v79 offset:1024\n"
    "ds_read_b64_tr_b16 v[14:15], v79 offset:1536\n"
    "ds_read_b64_tr_b16 v[16:17], v78 offset:2048\n"
    "ds_read_b64_tr_b16 v[18:19], v78 offset:2560\n"
    "ds_read_b64_tr_b16 v[20:21], v79 offset:2048\n"
    "ds_read_b64_tr_b16 v[22:23], v79 offset:2560\n"
    "ds_read_b64_tr_b16 v[24:25], v78 offset:3072\n"
    "ds_read_b64_tr_b16 v[26:27], v78 offset:3584\n"
    "ds_read_b64_tr_b16 v[28:29], v79 offset:3072\n"
    "ds_read_b64_tr_b16 v[30:31], v79 offset:3584\n"
    "v_mov_b32 v60, 0x3f803f80\n"
    "v_mov_b32 v61, 0x3f803f80\n"
    "v_mov_b32 v62, 0x3f803f80\n"
    "v_mov_b32 v63, 0x3f803f80\n"
    "v_mov_b32 v64, 0x3f803f80\n"
    "v_mov_b32 v65, 0x3f803f80\n"
    "v_mov_b32 v66, 0x3f803f80\n"
    "v_mov_b32 v67, 0x3f803f80\n"
    "v_mov_b32 v68, 0x3f803f80\n"
    "v_mov_b32 v69, 0x3f803f80\n"
    "v_mov_b32 v70, 0x3f803f80\n"
    "v_mov_b32 v71, 0x3f803f80\n"
    "v_mov_b32 v72, 0x3f803f80\n"
    "v_mov_b32 v73, 0x3f803f80\n"
    "v_mov_b32 v74, 0x3f803f80\n"
    "v_mov_b32 v75, 0x3f803f80\n"
    "v_and_b32 v76, 0xffff, v86\n"
    "v_lshrrev_b32 v78, 9, v95\n"
    "v_lshrrev_b32 v78, 9, v95\n"
    "s_mov_b32 s34, 0xfffcc000\n"
    "s_waitcnt lgkmcnt(0)\n"
    "s_call_b64 s[56:57], .Lwq_%=\n"
    "s_cmp_lt_i32 s89, s90\n"
    "s_cbranch_scc0 .L29_%=\n"
    "s_swappc_b64 s[96:97], s[94:95]\n"
    ".L29_%=:\n"
    "s_waitcnt lgkmcnt(0)\n"
    "s_barrier\n"
    "s_nop 1\n"
    "v_mov_b32 v88, v208\n"
    "v_mov_b32 v89, v209\n"
    "v_lshrrev_b32 v78, 9, v95\n"
    "s_mov_b32 s34, 0x9c000\n"
    "s_waitcnt lgkmcnt(0)\n"
    "s_call_b64 s[56:57], .Lwq_%=\n"
    "s_cmp_lt_i32 s89, s90\n"
    "s_cbranch_scc0 .L30_%=\n"
    "s_swappc_b64 s[96:97], s[94:95]\n"
    ".L30_%=:\n"
    "s_waitcnt lgkmcnt(0)\n"
    "s_barrier\n"
    "s_mov_b32 s44, 0x800000\n"
    "s_and_b32 s45, s20, 3\n"
    "s_lshl_b32 s45, s45, 12\n"
    "v_lshlrev_b32 v38, 3, v95\n"
    "v_add_u32 v38, s45, v38\n"
    "v_writelane_b32 v38, 0, 32\n"
    "v_lshrrev_b32 v39, 1, v95\n"
    "s_mov_b32 s45, 0x804080\n"
    ".L33_%=:\n"
    "s_mov_b32 exec_hi, 1\n"
    "buffer_load_dwordx2 v[36:37], v38, s[0:3], s44 offen sc1\n"
    "s_mov_b64 exec, -1\n"
    "buffer_load_dwordx2 v[42:43], v39, s[0:3], s45 offen sc1\n"
    "buffer_load_dwordx2 v[44:45], v39, s[0:3], s45 offen offset:512 sc1\n"
    "s_mov_b64 s[4:5], s[16:17]\n"
    "s_mov_b32 s6, 0x400000\n"
    "s_and_b32 s33, s20, 7\n"
    "s_bfe_u32 s35, s20, 0x40003\n"
    "s_lshl_b32 s35, s35, 2\n"
    "s_add_u32 s35, s35, s21\n"
    "s_lshl_b32 s33, s33, 19\n"
    "s_lshl_b32 s35, s35, 13\n"
    "v_lshlrev_b32 v54, 3, v95\n"
    "s_add_u32 s33, s33, s35\n"
    "s_mov_b32 exec_lo, 0\n"
    "buffer_load_dword v58, v54, s[4:7], s33 offen\n"
    "s_mov_b64 exec, -1\n"
    "s_waitcnt vmcnt(1)\n"
    "s_mov_b32 s98, 0\n"
    "v_xor_b32 v36, s69, v36\n"
    "v_xor_b32 v37, s37, v37\n"
    "v_or_b32 v36, v36, v37\n"
    "v_cmp_ne_u32 vcc, 0, v36\n"
    "s_and_b32 s98, vcc_hi, 1\n"
    "s_or_b32 s98, s98, vcc_lo\n"
    "v_xor_b32 v42, s69, v42\n"
    "v_xor_b32 v44, s69, v44\n"
    "v_or_b32 v42, v42, v44\n"
    "v_cmp_ne_u32 vcc, 0, v42\n"
    "s_or_b32 s98, s98, vcc_lo\n"
    "s_or_b32 s98, s98, vcc_hi\n"
    "s_cbranch_scc0 .L34_%=\n"
    "s_addk_i32 s41, -1\n"
    "s_cmp_gt_i32 s41, 0\n"
    "s_cbranch_scc0 .L34_%=\n"
    "s_sleep 8\n"
    "s_branch .L33_%=\n"
    ".L34_%=:\n"
    "v_readfirstlane_b32 s44, v43\n"
    "v_and_b32 v42, 0xfff, v43\n"
    "v_bfe_u32 v43, v43, 12, 12\n"
    "v_and_b32 v44, 0xfff, v45\n"
    "v_bfe_u32 v45, v45, 12, 12\n"
    "s_nop 0\n"
    "v_min_u32_dpp v44, v44, v44 quad_perm:[1,0,3,2] row_mask:0xf bank_mask:0xf\n"
    "v_min_u32_dpp v45, v45, v45 quad_perm:[1,0,3,2] row_mask:0xf bank_mask:0xf\n"
    "s_nop 0\n"
    "v_min_u32_dpp v44, v44, v44 quad_perm:[2,3,0,1] row_mask:0xf bank_mask:0xf\n"
    "v_min_u32_dpp v45, v45, v45 quad_perm:[2,3,0,1] row_mask:0xf bank_mask:0xf\n"
    "v_min_u32_dpp v42, v42, v42 row_ror:4 row_mask:0xf bank_mask:0xf\n"
    "v_min_u32_dpp v43, v43, v43 row_ror:4 row_mask:0xf bank_mask:0xf\n"
    "v_min_u32_dpp v44, v44, v44 row_ror:4 row_mask:0xf bank_mask:0xf\n"
    "v_min_u32_dpp v45, v45, v45 row_ror:4 row_mask:0xf bank_mask:0xf\n"
    "v_min_u32_dpp v42, v42, v42 row_ror:8 row_mask:0xf bank_mask:0xf\n"
    "v_min_u32_dpp v43, v43, v43 row_ror:8 row_mask:0xf bank_mask:0xf\n"
    "v_min_u32_dpp v44, v44, v44 row_ror:8 row_mask:0xf bank_mask:0xf\n"
    "v_min_u32_dpp v45, v45, v45 row_ror:8 row_mask:0xf bank_mask:0xf\n"
    "v_mov_b32 v46, v42\n"
    "v_mov_b32 v47, v43\n"
    "v_mov_b32 v48, v44\n"
    "v_mov_b32 v49, v45\n"
    "v_permlane16_swap_b32 v42, v46\n"
    "v_permlane16_swap_b32 v43, v47\n"
    "v_permlane16_swap_b32 v44, v48\n"
    "v_permlane16_swap_b32 v45, v49\n"
    "v_min_u32 v42, v42, v46\n"
    "v_min_u32 v43, v43, v47\n"
    "v_min_u32 v44, v44, v48\n"
    "v_min_u32 v45, v45, v49\n"
    "v_mov_b32 v46, v42\n"
    "v_mov_b32 v47, v43\n"
    "v_mov_b32 v48, v44\n"
    "v_mov_b32 v49, v45\n"
    "v_permlane32_swap_b32 v42, v46\n"
    "v_permlane32_swap_b32 v43, v47\n"
    "v_permlane32_swap_b32 v44, v48\n"
    "v_permlane32_swap_b32 v45, v49\n"
    "v_min_u32 v42, v42, v46\n"
    "v_min_u32 v43, v43, v47\n"
    "v_min_u32 v44, v44, v48\n"
    "v_min_u32 v45, v45, v49\n"
    "v_cmp_gt_u32 vcc, v42, v43\n"
    "s_mov_b32 s46, vcc_lo\n"
    "v_cmp_gt_u32 vcc, v44, v45\n"
    "s_mov_b32 s71, vcc_lo\n"
    "s_lshr_b32 s44, s44, 24\n"
    "s_and_b32 s45, s20, 7\n"
    "s_add_u32 s44, s45, s44\n"
    "s_lshr_b32 s44, s44, 2\n"
    "s_lshr_b32 s45, s20, 3\n"
    "s_add_u32 s98, s72, s66\n"
    "s_xor_b32 s98, s98, s72\n"
    "s_lshr_b32 s98, s98, 21\n"
    "s_lshr_b32 s100, s46, 0\n"
    "s_xor_b32 s98, s98, s100\n"
    "s_add_u32 s99, s76, s66\n"
    "s_xor_b32 s99, s99, s76\n"
    "s_lshr_b32 s99, s99, 21\n"
    "s_lshr_b32 s100, s46, 1\n"
    "s_xor_b32 s99, s99, s100\n"
    "s_xor_b32 s99, s99, s98\n"
    "s_and_b32 s99, s99, s45\n"
    "s_xor_b32 s98, s98, s99\n"
    "s_xor_b32 s98, s98, s44\n"
    "s_and_b32 s98, s98, 1\n"
    "s_lshl_b32 s61, s98, 13\n"
    "s_add_u32 s98, s80, s66\n"
    "s_xor_b32 s98, s98, s80\n"
    "s_lshr_b32 s98, s98, 21\n"
    "s_lshr_b32 s100, s46, 2\n"
    "s_xor_b32 s98, s98, s100\n"
    "s_add_u32 s99, s84, s66\n"
    "s_xor_b32 s99, s99, s84\n"
    "s_lshr_b32 s99, s99, 21\n"
    "s_lshr_b32 s100, s46, 3\n"
    "s_xor_b32 s99, s99, s100\n"
    "s_xor_b32 s99, s99, s98\n"
    "s_and_b32 s99, s99, s45\n"
    "s_xor_b32 s98, s98, s99\n"
    "s_xor_b32 s98, s98, s44\n"
    "s_and_b32 s98, s98, 1\n"
    "s_lshl_b32 s62, s98, 13\n"
    "s_xor_b32 s71, s71, s44\n"
    "s_lshr_b32 s98, s20, 2\n"
    "s_lshr_b32 s99, s20, 5\n"
    "s_xor_b32 s98, s98, s99\n"
    "s_xor_b32 s44, s71, s98\n"
    "s_xor_b32 s45, s71, 1\n"
    "s_lshr_b32 s98, s22, 13\n"
    "s_xor_b32 s98, s98, s71\n"
    "s_and_b32 s98, s98, 1\n"
    "s_lshl_b32 s98, s98, 21\n"
    "s_or_b32 s22, s22, s98\n"
    "s_lshr_b32 s98, s26, 13\n"
    "s_xor_b32 s98, s98, s71\n"
    "s_and_b32 s98, s98, 1\n"
    "s_lshl_b32 s98, s98, 21\n"
    "s_or_b32 s26, s26, s98\n"
    "s_lshr_b32 s98, s27, 13\n"
    "s_xor_b32 s98, s98, s71\n"
    "s_and_b32 s98, s98, 1\n"
    "s_lshl_b32 s98, s98, 21\n"
    "s_or_b32 s27, s27, s98\n"
    "s_lshr_b32 s98, s29, 13\n"
    "s_xor_b32 s98, s98, s71\n"
    "s_and_b32 s98, s98, 1\n"
    "s_lshl_b32 s98, s98, 21\n"
    "s_or_b32 s29, s29, s98\n"
    "s_lshr_b32 s98, s23, 13\n"
    "s_xor_b32 s98, s98, s44\n"
    "s_and_b32 s98, s98, 1\n"
    "s_lshl_b32 s98, s98, 21\n"
    "s_or_b32 s23, s23, s98\n"
    "s_lshr_b32 s98, s24, 13\n"
    "s_xor_b32 s98, s98, s44\n"
    "s_and_b32 s98, s98, 1\n"
    "s_lshl_b32 s98, s98, 21\n"
    "s_or_b32 s24, s24, s98\n"
    "s_lshr_b32 s98, s28, 13\n"
    "s_xor_b32 s98, s98, s45\n"
    "s_and_b32 s98, s98, 1\n"
    "s_lshl_b32 s98, s98, 21\n"
    "s_or_b32 s28, s28, s98\n"
    "s_lshr_b32 s98, s30, 13\n"
    "s_xor_b32 s98, s98, s45\n"
    "s_and_b32 s98, s98, 1\n"
    "s_lshl_b32 s98, s98, 21\n"
    "s_or_b32 s30, s30, s98\n"
    "s_mov_b32 s63, s61\n"
    "s_mov_b32 s70, -1\n"
    "s_call_b64 s[56:57], .Lown_%=\n"
    "s_mov_b64 s[4:5], s[14:15]\n"
    "s_mov_b32 s6, 0x400000\n"
    "s_and_b32 s44, s20, 7\n"
    "s_bfe_u32 s45, s20, 0x40003\n"
    "s_lshl_b32 s45, s45, 2\n"
    "s_add_u32 s45, s45, s21\n"
    "s_lshl_b32 s44, s44, 10\n"
    "s_lshl_b32 s45, s45, 16\n"
    "v_lshrrev_b32 v54, 7, v95\n"
    "v_lshlrev_b32 v54, 13, v54\n"
    "v_and_b32 v55, 0x70, v95\n"
    "v_lshl_or_b32 v54, v55, 3, v54\n"
    "s_add_u32 s44, s44, s45\n"
    "s_mov_b32 exec_hi, 0\n"
    "buffer_load_dword v58, v54, s[4:7], s44 offen\n"
    "s_mov_b64 exec, -1\n"
    "s_nop 1\n"
    "v_mov_b32 v88, v90\n"
    "v_mov_b32 v89, v91\n"
    "v_lshrrev_b32 v78, 9, v95\n"
    "s_mov_b32 s34, 0x49c000\n"
    "s_waitcnt lgkmcnt(0)\n"
    "s_call_b64 s[56:57], .Lwq_%=\n"
    "s_cmp_lt_i32 s89, s90\n"
    "s_cbranch_scc0 .L31_%=\n"
    "s_swappc_b64 s[96:97], s[94:95]\n"
    ".L31_%=:\n"
    "s_waitcnt lgkmcnt(0)\n"
    "s_barrier\n"
    "s_waitcnt vmcnt(0)\n"
    "s_mov_b64 s[4:5], s[16:17]\n"
    "s_mov_b32 s6, 0x400000\n"
    "v_lshrrev_b32 v56, 8, v80\n"
    "v_lshlrev_b32 v56, 10, v56\n"
    "v_and_or_b32 v56, v80, 48, v56\n"
    "s_add_u32 m0, s54, 0x0\n"
    "s_add_u32 s71, s32, 0x0\n"
    "buffer_load_dwordx4 v56, s[4:7], s71 offen lds\n"
    "s_add_u32 m0, s54, 0x400\n"
    "s_add_u32 s71, s32, 0x1000\n"
    "buffer_load_dwordx4 v56, s[4:7], s71 offen lds\n"
    "s_add_u32 m0, s54, 0x800\n"
    "s_add_u32 s71, s32, 0x8000\n"
    "buffer_load_dwordx4 v56, s[4:7], s71 offen lds\n"
    "s_add_u32 m0, s54, 0xc00\n"
    "s_add_u32 s71, s32, 0x9000\n"
    "buffer_load_dwordx4 v56, s[4:7], s71 offen lds\n"
    "s_add_u32 m0, s54, 0x1000\n"
    "s_add_u32 s71, s32, 0x10000\n"
    "buffer_load_dwordx4 v56, s[4:7], s71 offen lds\n"
    "s_add_u32 m0, s54, 0x1400\n"
    "s_add_u32 s71, s32, 0x11000\n"
    "buffer_load_dwordx4 v56, s[4:7], s71 offen lds\n"
    "s_add_u32 m0, s54, 0x1800\n"
    "s_add_u32 s71, s32, 0x18000\n"
    "buffer_load_dwordx4 v56, s[4:7], s71 offen lds\n"
    "s_add_u32 m0, s54, 0x1c00\n"
    "s_add_u32 s71, s32, 0x19000\n"
    "buffer_load_dwordx4 v56, s[4:7], s71 offen lds\n"
    "s_mov_b64 s[4:5], s[14:15]\n"
    "s_mov_b32 s6, 0x400000\n"
    "s_and_b32 s44, s20, 7\n"
    "s_bfe_u32 s45, s20, 0x40003\n"
    "s_lshl_b32 s45, s45, 2\n"
    "s_add_u32 s45, s45, s21\n"
    "s_lshl_b32 s44, s44, 10\n"
    "s_lshl_b32 s45, s45, 16\n"
    "v_lshrrev_b32 v54, 7, v95\n"
    "v_lshlrev_b32 v54, 13, v54\n"
    "v_and_b32 v55, 0x70, v95\n"
    "v_lshl_or_b32 v54, v55, 3, v54\n"
    "s_add_u32 s44, s44, s45\n"
    "s_mov_b32 exec_lo, 0\n"
    "buffer_load_dword v58, v54, s[4:7], s44 offen\n"
    "s_mov_b64 exec, -1\n"
    "s_nop 1\n"
    "v_mov_b32 v88, v210\n"
    "v_mov_b32 v89, v211\n"
    "v_lshrrev_b32 v78, 9, v95\n"
    "s_mov_b32 s34, 0x3cc000\n"
    "s_waitcnt lgkmcnt(0)\n"
    "s_call_b64 s[56:57], .Lwq_%=\n"
    "s_cmp_lt_i32 s89, s90\n"
    "s_cbranch_scc0 .L32_%=\n"
    "s_swappc_b64 s[96:97], s[94:95]\n"
    ".L32_%=:\n"
    "s_waitcnt lgkmcnt(0)\n"
    "s_barrier\n"
    "s_mov_b64 s[4:5], s[18:19]\n"
    "s_mov_b32 s6, 0x400000\n"
    "s_and_b32 s44, s20, 7\n"
    "s_bfe_u32 s45, s20, 0x40003\n"
    "s_lshl_b32 s45, s45, 2\n"
    "s_add_u32 s45, s45, s21\n"
    "s_lshl_b32 s44, s44, 19\n"
    "s_lshl_b32 s45, s45, 13\n"
    "v_lshlrev_b32 v54, 3, v95\n"
    "s_add_u32 s44, s44, s45\n"
    "s_mov_b32 exec_hi, 0\n"
    "buffer_load_dword v58, v54, s[4:7], s44 offen\n"
    "s_mov_b64 exec, -1\n"
    "s_sleep 8\n"
    "s_mov_b64 s[4:5], s[18:19]\n"
    "s_mov_b32 s6, 0x400000\n"
    "s_and_b32 s44, s20, 7\n"
    "s_bfe_u32 s45, s20, 0x40003\n"
    "s_lshl_b32 s45, s45, 2\n"
    "s_add_u32 s45, s45, s21\n"
    "s_lshl_b32 s44, s44, 19\n"
    "s_lshl_b32 s45, s45, 13\n"
    "v_lshlrev_b32 v54, 3, v95\n"
    "s_add_u32 s44, s44, s45\n"
    "s_mov_b32 exec_lo, 0\n"
    "buffer_load_dword v58, v54, s[4:7], s44 offen\n"
    "s_mov_b64 exec, -1\n"
    "s_mov_b32 s58, s47\n"
    "s_mov_b32 s70, 9000\n"
    "s_or_b32 s88, s66, s61\n"
    "s_andn2_b32 s68, s88, 0x800\n"
    "s_waitcnt lgkmcnt(0)\n"
    "s_add_u32 s99, s58, s70\n"
    ".L35_%=:\n"
    "s_memtime s[100:101]\n"
    "s_waitcnt lgkmcnt(0)\n"
    "s_sub_u32 s98, s100, s99\n"
    "s_cmp_lt_i32 s98, 0\n"
    "s_cbranch_scc1 .L35_%=\n"
    "s_memrealtime s[100:101]\n"
    "s_waitcnt lgkmcnt(0)\n"
    "s_mov_b32 s65, s100\n"
    "s_addk_i32 s90, 32\n"
    "s_swappc_b64 s[96:97], s[94:95]\n"
    "s_swappc_b64 s[96:97], s[94:95]\n"
    "s_swappc_b64 s[96:97], s[94:95]\n"
    "s_swappc_b64 s[96:97], s[94:95]\n"
    "s_swappc_b64 s[96:97], s[94:95]\n"
    "s_branch .L3_%=\n"
    ".L43_%=:\n"
    "s_add_i32 s98, s89, 5\n"
    "s_cmp_le_i32 s98, s90\n"
    "s_cbranch_scc0 .L45_%=\n"
    "s_waitcnt vmcnt(10)\n"
    "s_swappc_b64 s[96:97], s[94:95]\n"
    "s_waitcnt vmcnt(10)\n"
    "s_swappc_b64 s[96:97], s[94:95]\n"
    "s_waitcnt vmcnt(10)\n"
    "s_swappc_b64 s[96:97], s[94:95]\n"
    "s_waitcnt vmcnt(10)\n"
    "s_swappc_b64 s[96:97], s[94:95]\n"
    "s_waitcnt vmcnt(10)\n"
    "s_swappc_b64 s[96:97], s[94:95]\n"
    "s_waitcnt vmcnt(10)\n"
    "s_sub_i32 s98, s89, 5\n"
    "s_max_i32 s91, s91, s98\n"
    "s_branch .L44_%=\n"
    ".L45_%=:\n"
    "s_mov_b32 s99, s89\n"
    ".L46_%=:\n"
    "s_swappc_b64 s[96:97], s[94:95]\n"
    "s_cmp_lt_i32 s89, s90\n"
    "s_cbranch_scc1 .L46_%=\n"
    "s_max_i32 s91, s91, s99\n"
    "s_sub_i32 s98, s89, s99\n"
    "s_waitcnt vmcnt(8)\n"
    "s_cmp_ge_i32 s98, 4\n"
    "s_cbranch_scc1 .L44_%=\n"
    "s_waitcnt vmcnt(6)\n"
    "s_cmp_ge_i32 s98, 3\n"
    "s_cbranch_scc1 .L44_%=\n"
    "s_waitcnt vmcnt(4)\n"
    "s_cmp_ge_i32 s98, 2\n"
    "s_cbranch_scc1 .L44_%=\n"
    "s_waitcnt vmcnt(2)\n"
    "s_branch .L44_%=\n"
    ".L47_%=:\n"
    "s_add_i32 s98, s89, 5\n"
    "s_cmp_le_i32 s98, s90\n"
    "s_cbranch_scc0 .L49_%=\n"
    "s_waitcnt vmcnt(10)\n"
    "s_swappc_b64 s[96:97], s[94:95]\n"
    "s_waitcnt vmcnt(10)\n"
    "s_swappc_b64 s[96:97], s[94:95]\n"
    "s_waitcnt vmcnt(10)\n"
    "s_swappc_b64 s[96:97], s[94:95]\n"
    "s_waitcnt vmcnt(10)\n"
    "s_swappc_b64 s[96:97], s[94:95]\n"
    "s_waitcnt vmcnt(10)\n"
    "s_swappc_b64 s[96:97], s[94:95]\n"
    "s_waitcnt vmcnt(10)\n"
    "s_sub_i32 s98, s89, 5\n"
    "s_max_i32 s91, s91, s98\n"
    "s_branch .L48_%=\n"
    ".L49_%=:\n"
    "s_mov_b32 s99, s89\n"
    ".L50_%=:\n"
    "s_swappc_b64 s[96:97], s[94:95]\n"
    "s_cmp_lt_i32 s89, s90\n"
    "s_cbranch_scc1 .L50_%=\n"
    "s_max_i32 s91, s91, s99\n"
    "s_sub_i32 s98, s89, s99\n"
    "s_waitcnt vmcnt(8)\n"
    "s_cmp_ge_i32 s98, 4\n"
    "s_cbranch_scc1 .L48_%=\n"
    "s_waitcnt vmcnt(6)\n"
    "s_cmp_ge_i32 s98, 3\n"
    "s_cbranch_scc1 .L48_%=\n"
    "s_waitcnt vmcnt(4)\n"
    "s_cmp_ge_i32 s98, 2\n"
    "s_cbranch_scc1 .L48_%=\n"
    "s_waitcnt vmcnt(2)\n"
    "s_branch .L48_%=\n"
    ".L39_%=:\n"
    "buffer_load_dwordx4 v[0:3], v18, s[0:3], s45 offen sc1\n"
    "buffer_load_dwordx4 v[4:7], v18, s[0:3], s45 offen offset:256 sc1\n"
    "s_cmp_lt_i32 s89, s90\n"
    "s_cbranch_scc1 .L47_%=\n"
    "s_waitcnt vmcnt(0)\n"
    "s_mov_b32 s91, s89\n"
    ".L48_%=:\n"
    "s_branch .Latt2_%=\n"
    ".L52_%=:\n"
    "s_addk_i32 s41, -1\n"
    "s_cmp_gt_i32 s41, 0\n"
    "s_cbranch_scc0 .L51_%=\n"
    "s_sleep 2\n"
    "s_branch .L39_%=\n"
    ".L53_%=:\n"
    "s_addk_i32 s90, 4\n"
    "s_swappc_b64 s[96:97], s[94:95]\n"
    "s_swappc_b64 s[96:97], s[94:95]\n"
    "s_swappc_b64 s[96:97], s[94:95]\n"
    "s_swappc_b64 s[96:97], s[94:95]\n"
    "s_branch .L54_%=\n"
    ".L55_%=:\n"
    "s_addk_i32 s90, 4\n"
    "s_branch .L56_%=\n"
    ".L57_%=:\n"
    "s_swappc_b64 s[96:97], s[94:95]\n"
    "s_branch .L58_%=\n"
    ".L59_%=:\n"
    "s_swappc_b64 s[96:97], s[94:95]\n"
    "s_branch .L60_%=\n"
    ".L61_%=:\n"
    "s_swappc_b64 s[96:97], s[94:95]\n"
    "s_branch .L62_%=\n"
    ".L67_%=:\n"
    "s_cmp_lt_i32 s89, s90\n"
    "s_cbranch_scc0 .L69_%=\n"
    "s_waitcnt vmcnt(8)\n"
    "s_sub_i32 s98, s89, 4\n"
    "s_max_i32 s91, s91, s98\n"
    "s_swappc_b64 s[96:97], s[94:95]\n"
    "s_cmp_lt_i32 s91, s44\n"
    "s_cbranch_scc1 .L67_%=\n"
    "s_branch .L68_%=\n"
    ".L69_%=:\n"
    "s_waitcnt vmcnt(8)\n"
    "s_sub_i32 s98, s89, 4\n"
    "s_max_i32 s91, s91, s98\n"
    "s_cmp_ge_i32 s91, s44\n"
    "s_cbranch_scc1 .L68_%=\n"
    "s_waitcnt vmcnt(0)\n"
    "s_mov_b32 s91, s89\n"
    "s_branch .L68_%=\n"
    ".L65_%=:\n"
    "s_add_i32 s44, s36, -12\n"
    "s_cmp_lt_i32 s91, s44\n"
    "s_cbranch_scc1 .L67_%=\n"
    ".L68_%=:\n"
    "ds_read_b128 v[40:43], v37 offset:0\n"
    "ds_read_b128 v[44:47], v37 offset:1024\n"
    "ds_read_b128 v[48:51], v37 offset:2048\n"
    "ds_read_b128 v[52:55], v37 offset:3072\n"
    "ds_read_b128 v[56:59], v37 offset:4096\n"
    "ds_read_b128 v[60:63], v37 offset:5120\n"
    "ds_read_b128 v[64:67], v37 offset:6144\n"
    "ds_read_b128 v[68:71], v37 offset:7168\n"
    "s_waitcnt lgkmcnt(0)\n"
    "v_perm_b32 v78, v44, v40, s38\n"
    "v_perm_b32 v44, v44, v40, s39\n"
    "v_mov_b32 v40, v78\n"
    "v_perm_b32 v78, v53, v49, s38\n"
    "v_perm_b32 v53, v53, v49, s39\n"
    "v_mov_b32 v49, v78\n"
    "v_perm_b32 v78, v62, v58, s38\n"
    "v_perm_b32 v62, v62, v58, s39\n"
    "v_mov_b32 v58, v78\n"
    "v_perm_b32 v78, v71, v67, s38\n"
    "v_perm_b32 v71, v71, v67, s39\n"
    "v_mov_b32 v67, v78\n"
    "v_perm_b32 v78, v45, v41, s38\n"
    "v_perm_b32 v79, v45, v41, s39\n"
    "v_perm_b32 v41, v52, v48, s38\n"
    "v_perm_b32 v45, v52, v48, s39\n"
    "v_mov_b32 v48, v78\n"
    "v_mov_b32 v52, v79\n"
    "v_perm_b32 v78, v46, v42, s38\n"
    "v_perm_b32 v79, v46, v42, s39\n"
    "v_perm_b32 v42, v60, v56, s38\n"
    "v_perm_b32 v46, v60, v56, s39\n"
    "v_mov_b32 v56, v78\n"
    "v_mov_b32 v60, v79\n"
    "v_perm_b32 v78, v47, v43, s38\n"
    "v_perm_b32 v79, v47, v43, s39\n"
    "v_perm_b32 v43, v68, v64, s38\n"
    "v_perm_b32 v47, v68, v64, s39\n"
    "v_mov_b32 v64, v78\n"
    "v_mov_b32 v68, v79\n"
    "v_perm_b32 v78, v54, v50, s38\n"
    "v_perm_b32 v79, v54, v50, s39\n"
    "v_perm_b32 v50, v61, v57, s38\n"
    "v_perm_b32 v54, v61, v57, s39\n"
    "v_mov_b32 v57, v78\n"
    "v_mov_b32 v61, v79\n"
    "v_perm_b32 v78, v55, v51, s38\n"
    "v_perm_b32 v79, v55, v51, s39\n"
    "v_perm_b32 v51, v69, v65, s38\n"
    "v_perm_b32 v55, v69, v65, s39\n"
    "v_mov_b32 v65, v78\n"
    "v_mov_b32 v69, v79\n"
    "v_perm_b32 v78, v63, v59, s38\n"
    "v_perm_b32 v79, v63, v59, s39\n"
    "v_perm_b32 v59, v70, v66, s38\n"
    "v_perm_b32 v63, v70, v66, s39\n"
    "v_mov_b32 v66, v78\n"
    "v_mov_b32 v70, v79\n"
    "s_branch .L66_%=\n"
    ".L70_%=:\n"
    "s_add_i32 s44, s36, -4\n"
    ".L72_%=:\n"
    "s_cmp_ge_i32 s89, s44\n"
    "s_cbranch_scc1 .L73_%=\n"
    "s_waitcnt vmcnt(8)\n"
    "s_swappc_b64 s[96:97], s[94:95]\n"
    "s_branch .L72_%=\n"
    ".L73_%=:\n"
    "s_cmp_lt_i32 s89, s90\n"
    "s_cbranch_scc0 .L74_%=\n"
    "s_swappc_b64 s[96:97], s[94:95]\n"
    ".L74_%=:\n"
    "s_cmp_lt_i32 s89, s90\n"
    "s_cbranch_scc0 .L75_%=\n"
    "s_swappc_b64 s[96:97], s[94:95]\n"
    ".L75_%=:\n"
    "s_cmp_lt_i32 s89, s90\n"
    "s_cbranch_scc0 .L76_%=\n"
    "s_swappc_b64 s[96:97], s[94:95]\n"
    ".L76_%=:\n"
    "s_cmp_lt_i32 s89, s90\n"
    "s_cbranch_scc0 .L77_%=\n"
    "s_swappc_b64 s[96:97], s[94:95]\n"
    ".L77_%=:\n"
    "s_waitcnt vmcnt(16)\n"
    "v_readfirstlane_b32 s98, v77\n"
    "s_add_u32 s46, s34, 0x2d000\n"
    "v_mov_b32 v77, 1\n"
    "s_cmp_eq_u32 s98, 14\n"
    "s_cbranch_scc0 .L79_%=\n"
    "s_mov_b64 exec, 1\n"
    "buffer_atomic_add v77, off, s[0:3], s46 sc0\n"
    "s_mov_b64 exec, -1\n"
    ".L79_%=:\n"
    "v_perm_b32 v78, v164, v160, s38\n"
    "v_perm_b32 v164, v164, v160, s39\n"
    "v_mov_b32 v160, v78\n"
    "v_perm_b32 v78, v173, v169, s38\n"
    "v_perm_b32 v173, v173, v169, s39\n"
    "v_mov_b32 v169, v78\n"
    "v_perm_b32 v78, v182, v178, s38\n"
    "v_perm_b32 v182, v182, v178, s39\n"
    "v_mov_b32 v178, v78\n"
    "v_perm_b32 v78, v191, v187, s38\n"
    "v_perm_b32 v191, v191, v187, s39\n"
    "v_mov_b32 v187, v78\n"
    "v_perm_b32 v78, v165, v161, s38\n"
    "v_perm_b32 v79, v165, v161, s39\n"
    "v_perm_b32 v161, v172, v168, s38\n"
    "v_perm_b32 v165, v172, v168, s39\n"
    "v_mov_b32 v168, v78\n"
    "v_mov_b32 v172, v79\n"
    "v_perm_b32 v78, v166, v162, s38\n"
    "v_perm_b32 v79, v166, v162, s39\n"
    "v_perm_b32 v162, v180, v176, s38\n"
    "v_perm_b32 v166, v180, v176, s39\n"
    "v_mov_b32 v176, v78\n"
    "v_mov_b32 v180, v79\n"
    "v_perm_b32 v78, v167, v163, s38\n"
    "v_perm_b32 v79, v167, v163, s39\n"
    "v_perm_b32 v163, v188, v184, s38\n"
    "v_perm_b32 v167, v188, v184, s39\n"
    "v_mov_b32 v184, v78\n"
    "v_mov_b32 v188, v79\n"
    "v_perm_b32 v78, v174, v170, s38\n"
    "v_perm_b32 v79, v174, v170, s39\n"
    "v_perm_b32 v170, v181, v177, s38\n"
    "v_perm_b32 v174, v181, v177, s39\n"
    "v_mov_b32 v177, v78\n"
    "v_mov_b32 v181, v79\n"
    "v_perm_b32 v78, v175, v171, s38\n"
    "v_perm_b32 v79, v175, v171, s39\n"
    "v_perm_b32 v171, v189, v185, s38\n"
    "v_perm_b32 v175, v189, v185, s39\n"
    "v_mov_b32 v185, v78\n"
    "v_mov_b32 v189, v79\n"
    "v_perm_b32 v78, v183, v179, s38\n"
    "v_perm_b32 v79, v183, v179, s39\n"
    "v_perm_b32 v179, v190, v186, s38\n"
    "v_perm_b32 v183, v190, v186, s39\n"
    "v_mov_b32 v186, v78\n"
    "v_mov_b32 v190, v79\n"
    "s_branch .L71_%=\n"
    ".L80_%=:\n"
    "s_swappc_b64 s[96:97], s[94:95]\n"
    "s_branch .L81_%=\n"
    ".L82_%=:\n"
    "s_waitcnt vmcnt(8)\n"
    "s_cmp_lt_i32 s64, 0\n"
    "s_cbranch_scc1 .L84_%=\n"
    "v_and_b32 v38, 0xffff, v84\n"
    "v_lshrrev_b32 v39, 16, v84\n"
    "s_add_u32 s44, s64, s26\n"
    "s_add_u32 s46, s64, s27\n"
    "buffer_load_dword v60, v39, s[0:3], s46 offen sc1\n"
    "buffer_load_dwordx4 v[44:47], v38, s[0:3], s44 offen offset:0 sc1\n"
    "buffer_load_dwordx4 v[48:51], v38, s[0:3], s44 offen offset:1024 sc1\n"
    "buffer_load_dwordx4 v[52:55], v38, s[0:3], s44 offen offset:2048 sc1\n"
    "buffer_load_dwordx4 v[56:59], v38, s[0:3], s44 offen offset:3072 sc1\n"
    ".L84_%=:\n"
    "v_perm_b32 v78, v196, v192, s38\n"
    "v_perm_b32 v196, v196, v192, s39\n"
    "v_mov_b32 v192, v78\n"
    "v_perm_b32 v78, v205, v201, s38\n"
    "v_perm_b32 v205, v205, v201, s39\n"
    "v_mov_b32 v201, v78\n"
    "v_perm_b32 v78, v214, v210, s38\n"
    "v_perm_b32 v214, v214, v210, s39\n"
    "v_mov_b32 v210, v78\n"
    "v_perm_b32 v78, v223, v219, s38\n"
    "v_perm_b32 v223, v223, v219, s39\n"
    "v_mov_b32 v219, v78\n"
    "v_perm_b32 v78, v197, v193, s38\n"
    "v_perm_b32 v79, v197, v193, s39\n"
    "v_perm_b32 v193, v204, v200, s38\n"
    "v_perm_b32 v197, v204, v200, s39\n"
    "v_mov_b32 v200, v78\n"
    "v_mov_b32 v204, v79\n"
    "v_perm_b32 v78, v198, v194, s38\n"
    "v_perm_b32 v79, v198, v194, s39\n"
    "v_perm_b32 v194, v212, v208, s38\n"
    "v_perm_b32 v198, v212, v208, s39\n"
    "v_mov_b32 v208, v78\n"
    "v_mov_b32 v212, v79\n"
    "v_perm_b32 v78, v199, v195, s38\n"
    "v_perm_b32 v79, v199, v195, s39\n"
    "v_perm_b32 v195, v220, v216, s38\n"
    "v_perm_b32 v199, v220, v216, s39\n"
    "v_mov_b32 v216, v78\n"
    "v_mov_b32 v220, v79\n"
    "v_perm_b32 v78, v206, v202, s38\n"
    "v_perm_b32 v79, v206, v202, s39\n"
    "v_perm_b32 v202, v213, v209, s38\n"
    "v_perm_b32 v206, v213, v209, s39\n"
    "v_mov_b32 v209, v78\n"
    "v_mov_b32 v213, v79\n"
    "v_perm_b32 v78, v207, v203, s38\n"
    "v_perm_b32 v79, v207, v203, s39\n"
    "v_perm_b32 v203, v221, v217, s38\n"
    "v_perm_b32 v207, v221, v217, s39\n"
    "v_mov_b32 v217, v78\n"
    "v_mov_b32 v221, v79\n"
    "v_perm_b32 v78, v215, v211, s38\n"
    "v_perm_b32 v79, v215, v211, s39\n"
    "v_perm_b32 v211, v222, v218, s38\n"
    "v_perm_b32 v215, v222, v218, s39\n"
    "v_mov_b32 v218, v78\n"
    "v_mov_b32 v222, v79\n"
    "s_branch .L83_%=\n"
    ".L85_%=:\n"
    "s_waitcnt vmcnt(5)\n"
    "s_cmp_ge_i32 s64, 0\n"
    "s_cbranch_scc1 .L88_%=\n"
    "s_waitcnt vmcnt(0)\n"
    ".L88_%=:\n"
    "s_mov_b32 s91, s89\n"
    "v_readfirstlane_b32 s98, v77\n"
    "s_add_u32 s44, s34, 0x2d080\n"
    "s_cmp_eq_u32 s98, 30\n"
    "s_cbranch_scc0 .L87_%=\n"
    "s_memrealtime s[100:101]\n"
    "s_waitcnt lgkmcnt(0)\n"
    "s_min_u32 s100, s100, -2\n"
    "v_mov_b32 v78, s100\n"
    "s_mov_b64 exec, 1\n"
    "buffer_store_dword v78, off, s[0:3], s44 sc1\n"
    "s_mov_b64 exec, -1\n"
    ".L87_%=:\n"
    "v_perm_b32 v78, v228, v224, s38\n"
    "v_perm_b32 v228, v228, v224, s39\n"
    "v_mov_b32 v224, v78\n"
    "v_perm_b32 v78, v237, v233, s38\n"
    "v_perm_b32 v237, v237, v233, s39\n"
    "v_mov_b32 v233, v78\n"
    "v_perm_b32 v78, v246, v242, s38\n"
    "v_perm_b32 v246, v246, v242, s39\n"
    "v_mov_b32 v242, v78\n"
    "v_perm_b32 v78, v255, v251, s38\n"
    "v_perm_b32 v255, v255, v251, s39\n"
    "v_mov_b32 v251, v78\n"
    "v_perm_b32 v78, v229, v225, s38\n"
    "v_perm_b32 v79, v229, v225, s39\n"
    "v_perm_b32 v225, v236, v232, s38\n"
    "v_perm_b32 v229, v236, v232, s39\n"
    "v_mov_b32 v232, v78\n"
    "v_mov_b32 v236, v79\n"
    "v_perm_b32 v78, v230, v226, s38\n"
    "v_perm_b32 v79, v230, v226, s39\n"
    "v_perm_b32 v226, v244, v240, s38\n"
    "v_perm_b32 v230, v244, v240, s39\n"
    "v_mov_b32 v240, v78\n"
    "v_mov_b32 v244, v79\n"
    "v_perm_b32 v78, v231, v227, s38\n"
    "v_perm_b32 v79, v231, v227, s39\n"
    "v_perm_b32 v227, v252, v248, s38\n"
    "v_perm_b32 v231, v252, v248, s39\n"
    "v_mov_b32 v248, v78\n"
    "v_mov_b32 v252, v79\n"
    "v_perm_b32 v78, v238, v234, s38\n"
    "v_perm_b32 v79, v238, v234, s39\n"
    "v_perm_b32 v234, v245, v241, s38\n"
    "v_perm_b32 v238, v245, v241, s39\n"
    "v_mov_b32 v241, v78\n"
    "v_mov_b32 v245, v79\n"
    "v_perm_b32 v78, v239, v235, s38\n"
    "v_perm_b32 v79, v239, v235, s39\n"
    "v_perm_b32 v235, v253, v249, s38\n"
    "v_perm_b32 v239, v253, v249, s39\n"
    "v_mov_b32 v249, v78\n"
    "v_mov_b32 v253, v79\n"
    "v_perm_b32 v78, v247, v243, s38\n"
    "v_perm_b32 v79, v247, v243, s39\n"
    "v_perm_b32 v243, v254, v250, s38\n"
    "v_perm_b32 v247, v254, v250, s39\n"
    "v_mov_b32 v250, v78\n"
    "v_mov_b32 v254, v79\n"
    "s_branch .L86_%=\n"
    ".L90_%=:\n"
    "s_addk_i32 s41, -1\n"
    "s_cmp_gt_i32 s41, 0\n"
    "s_cbranch_scc0 .L89_%=\n"
    "s_sleep 2\n"
    "s_branch .L39_%=\n"
    ".L91_%=:\n"
    "s_addk_i32 s90, 4\n"
    "s_swappc_b64 s[96:97], s[94:95]\n"
    "s_swappc_b64 s[96:97], s[94:95]\n"
    "s_swappc_b64 s[96:97], s[94:95]\n"
    "s_swappc_b64 s[96:97], s[94:95]\n"
    "s_branch .L92_%=\n"
    ".L93_%=:\n"
    "s_addk_i32 s90, 4\n"
    "s_branch .L94_%=\n"
    ".L95_%=:\n"
    "s_swappc_b64 s[96:97], s[94:95]\n"
    "s_branch .L96_%=\n"
    ".L97_%=:\n"
    "s_swappc_b64 s[96:97], s[94:95]\n"
    "s_branch .L98_%=\n"
    ".L99_%=:\n"
    "s_swappc_b64 s[96:97], s[94:95]\n"
    "s_branch .L100_%=\n"
    ".L105_%=:\n"
    "s_cmp_lt_i32 s89, s90\n"
    "s_cbranch_scc0 .L107_%=\n"
    "s_waitcnt vmcnt(8)\n"
    "s_sub_i32 s98, s89, 4\n"
    "s_max_i32 s91, s91, s98\n"
    "s_swappc_b64 s[96:97], s[94:95]\n"
    "s_cmp_lt_i32 s91, s44\n"
    "s_cbranch_scc1 .L105_%=\n"
    "s_branch .L106_%=\n"
    ".L107_%=:\n"
    "s_waitcnt vmcnt(8)\n"
    "s_sub_i32 s98, s89, 4\n"
    "s_max_i32 s91, s91, s98\n"
    "s_cmp_ge_i32 s91, s44\n"
    "s_cbranch_scc1 .L106_%=\n"
    "s_waitcnt vmcnt(0)\n"
    "s_mov_b32 s91, s89\n"
    "s_branch .L106_%=\n"
    ".L103_%=:\n"
    "s_add_i32 s44, s36, -12\n"
    "s_cmp_lt_i32 s91, s44\n"
    "s_cbranch_scc1 .L105_%=\n"
    ".L106_%=:\n"
    "ds_read_b128 v[40:43], v37 offset:0\n"
    "ds_read_b128 v[44:47], v37 offset:1024\n"
    "ds_read_b128 v[48:51], v37 offset:2048\n"
    "ds_read_b128 v[52:55], v37 offset:3072\n"
    "ds_read_b128 v[56:59], v37 offset:4096\n"
    "ds_read_b128 v[60:63], v37 offset:5120\n"
    "ds_read_b128 v[64:67], v37 offset:6144\n"
    "ds_read_b128 v[68:71], v37 offset:7168\n"
    "s_waitcnt lgkmcnt(0)\n"
    "v_perm_b32 v78, v44, v40, s38\n"
    "v_perm_b32 v44, v44, v40, s39\n"
    "v_mov_b32 v40, v78\n"
    "v_perm_b32 v78, v53, v49, s38\n"
    "v_perm_b32 v53, v53, v49, s39\n"
    "v_mov_b32 v49, v78\n"
    "v_perm_b32 v78, v62, v58, s38\n"
    "v_perm_b32 v62, v62, v58, s39\n"
    "v_mov_b32 v58, v78\n"
    "v_perm_b32 v78, v71, v67, s38\n"
    "v_perm_b32 v71, v71, v67, s39\n"
    "v_mov_b32 v67, v78\n"
    "v_perm_b32 v78, v45, v41, s38\n"
    "v_perm_b32 v79, v45, v41, s39\n"
    "v_perm_b32 v41, v52, v48, s38\n"
    "v_perm_b32 v45, v52, v48, s39\n"
    "v_mov_b32 v48, v78\n"
    "v_mov_b32 v52, v79\n"
    "v_perm_b32 v78, v46, v42, s38\n"
    "v_perm_b32 v79, v46, v42, s39\n"
    "v_perm_b32 v42, v60, v56, s38\n"
    "v_perm_b32 v46, v60, v56, s39\n"
    "v_mov_b32 v56, v78\n"
    "v_mov_b32 v60, v79\n"
    "v_perm_b32 v78, v47, v43, s38\n"
    "v_perm_b32 v79, v47, v43, s39\n"
    "v_perm_b32 v43, v68, v64, s38\n"
    "v_perm_b32 v47, v68, v64, s39\n"
    "v_mov_b32 v64, v78\n"
    "v_mov_b32 v68, v79\n"
    "v_perm_b32 v78, v54, v50, s38\n"
    "v_perm_b32 v79, v54, v50, s39\n"
    "v_perm_b32 v50, v61, v57, s38\n"
    "v_perm_b32 v54, v61, v57, s39\n"
    "v_mov_b32 v57, v78\n"
    "v_mov_b32 v61, v79\n"
    "v_perm_b32 v78, v55, v51, s38\n"
    "v_perm_b32 v79, v55, v51, s39\n"
    "v_perm_b32 v51, v69, v65, s38\n"
    "v_perm_b32 v55, v69, v65, s39\n"
    "v_mov_b32 v65, v78\n"
    "v_mov_b32 v69, v79\n"
    "v_perm_b32 v78, v63, v59, s38\n"
    "v_perm_b32 v79, v63, v59, s39\n"
    "v_perm_b32 v59, v70, v66, s38\n"
    "v_perm_b32 v63, v70, v66, s39\n"
    "v_mov_b32 v66, v78\n"
    "v_mov_b32 v70, v79\n"
    "s_add_i32 s44, s36, -4\n"
    ".L108_%=:\n"
    "s_cmp_ge_i32 s89, s44\n"
    "s_cbranch_scc1 .L109_%=\n"
    "s_waitcnt vmcnt(8)\n"
    "s_swappc_b64 s[96:97], s[94:95]\n"
    "s_branch .L108_%=\n"
    ".L109_%=:\n"
    "s_cmp_lt_i32 s89, s90\n"
    "s_cbranch_scc0 .L110_%=\n"
    "s_swappc_b64 s[96:97], s[94:95]\n"
    ".L110_%=:\n"
    "s_cmp_lt_i32 s89, s90\n"
    "s_cbranch_scc0 .L111_%=\n"
    "s_swappc_b64 s[96:97], s[94:95]\n"
    ".L111_%=:\n"
    "s_cmp_lt_i32 s89, s90\n"
    "s_cbranch_scc0 .L112_%=\n"
    "s_swappc_b64 s[96:97], s[94:95]\n"
    ".L112_%=:\n"
    "s_cmp_lt_i32 s89, s90\n"
    "s_cbranch_scc0 .L113_%=\n"
    "s_swappc_b64 s[96:97], s[94:95]\n"
    ".L113_%=:\n"
    "s_waitcnt vmcnt(16)\n"
    "v_readfirstlane_b32 s98, v77\n"
    "s_add_u32 s46, s34, 0x2d000\n"
    "v_mov_b32 v77, 1\n"
    "s_cmp_eq_u32 s98, 14\n"
    "s_cbranch_scc0 .L115_%=\n"
    "s_mov_b64 exec, 1\n"
    "buffer_atomic_add v77, off, s[0:3], s46 sc0\n"
    "s_mov_b64 exec, -1\n"
    ".L115_%=:\n"
    "s_branch .L104_%=\n"
    ".L116_%=:\n"
    "s_swappc_b64 s[96:97], s[94:95]\n"
    "s_branch .L117_%=\n"
    ".L118_%=:\n"
    "s_waitcnt vmcnt(8)\n"
    "s_cmp_lt_i32 s64, 0\n"
    "s_cbranch_scc1 .L120_%=\n"
    "v_and_b32 v38, 0xffff, v84\n"
    "v_lshrrev_b32 v39, 16, v84\n"
    "s_add_u32 s44, s64, s26\n"
    "s_add_u32 s46, s64, s27\n"
    "buffer_load_dword v60, v39, s[0:3], s46 offen sc1\n"
    "buffer_load_dwordx4 v[44:47], v38, s[0:3], s44 offen offset:0 sc1\n"
    "buffer_load_dwordx4 v[48:51], v38, s[0:3], s44 offen offset:1024 sc1\n"
    "buffer_load_dwordx4 v[52:55], v38, s[0:3], s44 offen offset:2048 sc1\n"
    "buffer_load_dwordx4 v[56:59], v38, s[0:3], s44 offen offset:3072 sc1\n"
    ".L120_%=:\n"
    "v_perm_b32 v78, v100, v96, s38\n"
    "v_perm_b32 v100, v100, v96, s39\n"
    "v_mov_b32 v96, v78\n"
    "v_perm_b32 v78, v109, v105, s38\n"
    "v_perm_b32 v109, v109, v105, s39\n"
    "v_mov_b32 v105, v78\n"
    "v_perm_b32 v78, v118, v114, s38\n"
    "v_perm_b32 v118, v118, v114, s39\n"
    "v_mov_b32 v114, v78\n"
    "v_perm_b32 v78, v127, v123, s38\n"
    "v_perm_b32 v127, v127, v123, s39\n"
    "v_mov_b32 v123, v78\n"
    "v_perm_b32 v78, v101, v97, s38\n"
    "v_perm_b32 v79, v101, v97, s39\n"
    "v_perm_b32 v97, v108, v104, s38\n"
    "v_perm_b32 v101, v108, v104, s39\n"
    "v_mov_b32 v104, v78\n"
    "v_mov_b32 v108, v79\n"
    "v_perm_b32 v78, v102, v98, s38\n"
    "v_perm_b32 v79, v102, v98, s39\n"
    "v_perm_b32 v98, v116, v112, s38\n"
    "v_perm_b32 v102, v116, v112, s39\n"
    "v_mov_b32 v112, v78\n"
    "v_mov_b32 v116, v79\n"
    "v_perm_b32 v78, v103, v99, s38\n"
    "v_perm_b32 v79, v103, v99, s39\n"
    "v_perm_b32 v99, v124, v120, s38\n"
    "v_perm_b32 v103, v124, v120, s39\n"
    "v_mov_b32 v120, v78\n"
    "v_mov_b32 v124, v79\n"
    "v_perm_b32 v78, v110, v106, s38\n"
    "v_perm_b32 v79, v110, v106, s39\n"
    "v_perm_b32 v106, v117, v113, s38\n"
    "v_perm_b32 v110, v117, v113, s39\n"
    "v_mov_b32 v113, v78\n"
    "v_mov_b32 v117, v79\n"
    "v_perm_b32 v78, v111, v107, s38\n"
    "v_perm_b32 v79, v111, v107, s39\n"
    "v_perm_b32 v107, v125, v121, s38\n"
    "v_perm_b32 v111, v125, v121, s39\n"
    "v_mov_b32 v121, v78\n"
    "v_mov_b32 v125, v79\n"
    "v_perm_b32 v78, v119, v115, s38\n"
    "v_perm_b32 v79, v119, v115, s39\n"
    "v_perm_b32 v115, v126, v122, s38\n"
    "v_perm_b32 v119, v126, v122, s39\n"
    "v_mov_b32 v122, v78\n"
    "v_mov_b32 v126, v79\n"
    "s_branch .L119_%=\n"
    ".L121_%=:\n"
    "s_waitcnt vmcnt(5)\n"
    "s_cmp_ge_i32 s64, 0\n"
    "s_cbranch_scc1 .L124_%=\n"
    "s_waitcnt vmcnt(0)\n"
    ".L124_%=:\n"
    "s_mov_b32 s91, s89\n"
    "v_readfirstlane_b32 s98, v77\n"
    "s_add_u32 s44, s34, 0x2d080\n"
    "s_cmp_eq_u32 s98, 30\n"
    "s_cbranch_scc0 .L123_%=\n"
    "s_memrealtime s[100:101]\n"
    "s_waitcnt lgkmcnt(0)\n"
    "s_min_u32 s100, s100, -2\n"
    "v_mov_b32 v78, s100\n"
    "s_mov_b64 exec, 1\n"
    "buffer_store_dword v78, off, s[0:3], s44 sc1\n"
    "s_mov_b64 exec, -1\n"
    ".L123_%=:\n"
    "v_perm_b32 v78, v132, v128, s38\n"
    "v_perm_b32 v132, v132, v128, s39\n"
    "v_mov_b32 v128, v78\n"
    "v_perm_b32 v78, v141, v137, s38\n"
    "v_perm_b32 v141, v141, v137, s39\n"
    "v_mov_b32 v137, v78\n"
    "v_perm_b32 v78, v150, v146, s38\n"
    "v_perm_b32 v150, v150, v146, s39\n"
    "v_mov_b32 v146, v78\n"
    "v_perm_b32 v78, v159, v155, s38\n"
    "v_perm_b32 v159, v159, v155, s39\n"
    "v_mov_b32 v155, v78\n"
    "v_perm_b32 v78, v133, v129, s38\n"
    "v_perm_b32 v79, v133, v129, s39\n"
    "v_perm_b32 v129, v140, v136, s38\n"
    "v_perm_b32 v133, v140, v136, s39\n"
    "v_mov_b32 v136, v78\n"
    "v_mov_b32 v140, v79\n"
    "v_perm_b32 v78, v134, v130, s38\n"
    "v_perm_b32 v79, v134, v130, s39\n"
    "v_perm_b32 v130, v148, v144, s38\n"
    "v_perm_b32 v134, v148, v144, s39\n"
    "v_mov_b32 v144, v78\n"
    "v_mov_b32 v148, v79\n"
    "v_perm_b32 v78, v135, v131, s38\n"
    "v_perm_b32 v79, v135, v131, s39\n"
    "v_perm_b32 v131, v156, v152, s38\n"
    "v_perm_b32 v135, v156, v152, s39\n"
    "v_mov_b32 v152, v78\n"
    "v_mov_b32 v156, v79\n"
    "v_perm_b32 v78, v142, v138, s38\n"
    "v_perm_b32 v79, v142, v138, s39\n"
    "v_perm_b32 v138, v149, v145, s38\n"
    "v_perm_b32 v142, v149, v145, s39\n"
    "v_mov_b32 v145, v78\n"
    "v_mov_b32 v149, v79\n"
    "v_perm_b32 v78, v143, v139, s38\n"
    "v_perm_b32 v79, v143, v139, s39\n"
    "v_perm_b32 v139, v157, v153, s38\n"
    "v_perm_b32 v143, v157, v153, s39\n"
    "v_mov_b32 v153, v78\n"
    "v_mov_b32 v157, v79\n"
    "v_perm_b32 v78, v151, v147, s38\n"
    "v_perm_b32 v79, v151, v147, s39\n"
    "v_perm_b32 v147, v158, v154, s38\n"
    "v_perm_b32 v151, v158, v154, s39\n"
    "v_mov_b32 v154, v78\n"
    "v_mov_b32 v158, v79\n"
    "s_branch .L122_%=\n"
    ".L125_%=:\n"
    "s_swappc_b64 s[96:97], s[94:95]\n"
    "s_branch .L126_%=\n"
    ".L127_%=:\n"
    "s_cmp_eq_u32 s33, 7\n"
    "s_cbranch_scc1 .Lwop_%=\n"
    "s_add_u32 s44, s34, 0x2d080\n"
    "s_cmp_lg_u32 s21, 1\n"
    "s_cbranch_scc1 .L131_%=\n"
    "s_mov_b64 exec, 1\n"
    ".L130_%=:\n"
    "buffer_load_dword v61, off, s[0:3], s44 sc1\n"
    "s_waitcnt vmcnt(0)\n"
    "v_cmp_eq_u32 vcc, -1, v61\n"
    "s_cbranch_vccz .L132_%=\n"
    "s_addk_i32 s41, -1\n"
    "s_cmp_gt_i32 s41, 0\n"
    "s_cbranch_scc0 .L131_%=\n"
    "s_sleep 4\n"
    "s_branch .L130_%=\n"
    ".L132_%=:\n"
    "v_readfirstlane_b32 s99, v61\n"
    "s_add_u32 s99, s99, 65\n"
    "s_lshr_b32 s98, s20, 7\n"
    "s_sub_u32 s98, s33, s98\n"
    "s_cmp_eq_u32 s98, 2\n"
    "s_cselect_b32 s98, 60, 0\n"
    "s_add_u32 s99, s99, s98\n"
    "s_add_u32 s99, s99, s70\n"
    ".L133_%=:\n"
    "s_memrealtime s[100:101]\n"
    "s_waitcnt lgkmcnt(0)\n"
    "s_sub_u32 s98, s100, s99\n"
    "s_cmp_lt_i32 s98, 0\n"
    "s_cbranch_scc1 .L133_%=\n"
    ".L131_%=:\n"
    "s_mov_b64 exec, -1\n"
    "s_waitcnt lgkmcnt(0)\n"
    "s_barrier\n"
    "s_branch .L128_%=\n"
    ".L139_%=:\n"
    "s_add_i32 s98, s89, 5\n"
    "s_cmp_le_i32 s98, s90\n"
    "s_cbranch_scc0 .L143_%=\n"
    "s_waitcnt vmcnt(13)\n"
    "s_swappc_b64 s[96:97], s[94:95]\n"
    "s_waitcnt vmcnt(13)\n"
    "s_swappc_b64 s[96:97], s[94:95]\n"
    "s_waitcnt vmcnt(13)\n"
    "s_swappc_b64 s[96:97], s[94:95]\n"
    "s_waitcnt vmcnt(13)\n"
    "s_swappc_b64 s[96:97], s[94:95]\n"
    "s_waitcnt vmcnt(13)\n"
    "s_swappc_b64 s[96:97], s[94:95]\n"
    "s_waitcnt vmcnt(10)\n"
    "s_sub_i32 s98, s89, 5\n"
    "s_max_i32 s91, s91, s98\n"
    "s_branch .L141_%=\n"
    ".L143_%=:\n"
    "s_mov_b32 s99, s89\n"
    ".L144_%=:\n"
    "s_swappc_b64 s[96:97], s[94:95]\n"
    "s_cmp_lt_i32 s89, s90\n"
    "s_cbranch_scc1 .L144_%=\n"
    "s_max_i32 s91, s91, s99\n"
    "s_sub_i32 s98, s89, s99\n"
    "s_waitcnt vmcnt(8)\n"
    "s_cmp_ge_i32 s98, 4\n"
    "s_cbranch_scc1 .L141_%=\n"
    "s_waitcnt vmcnt(6)\n"
    "s_cmp_ge_i32 s98, 3\n"
    "s_cbranch_scc1 .L141_%=\n"
    "s_waitcnt vmcnt(4)\n"
    "s_cmp_ge_i32 s98, 2\n"
    "s_cbranch_scc1 .L141_%=\n"
    "s_waitcnt vmcnt(2)\n"
    "s_branch .L141_%=\n"
    ".L140_%=:\n"
    "s_add_i32 s98, s89, 5\n"
    "s_cmp_le_i32 s98, s90\n"
    "s_cbranch_scc0 .L145_%=\n"
    "s_waitcnt vmcnt(21)\n"
    "s_swappc_b64 s[96:97], s[94:95]\n"
    "s_waitcnt vmcnt(21)\n"
    "s_swappc_b64 s[96:97], s[94:95]\n"
    "s_waitcnt vmcnt(21)\n"
    "s_swappc_b64 s[96:97], s[94:95]\n"
    "s_waitcnt vmcnt(21)\n"
    "s_swappc_b64 s[96:97], s[94:95]\n"
    "s_waitcnt vmcnt(21)\n"
    "s_swappc_b64 s[96:97], s[94:95]\n"
    "s_waitcnt vmcnt(18)\n"
    "s_sub_i32 s98, s89, 5\n"
    "s_max_i32 s91, s91, s98\n"
    "s_branch .L141_%=\n"
    ".L145_%=:\n"
    "s_mov_b32 s99, s89\n"
    ".L146_%=:\n"
    "s_swappc_b64 s[96:97], s[94:95]\n"
    "s_cmp_lt_i32 s89, s90\n"
    "s_cbranch_scc1 .L146_%=\n"
    "s_max_i32 s91, s91, s99\n"
    "s_sub_i32 s98, s89, s99\n"
    "s_waitcnt vmcnt(16)\n"
    "s_cmp_ge_i32 s98, 4\n"
    "s_cbranch_scc1 .L141_%=\n"
    "s_waitcnt vmcnt(14)\n"
    "s_cmp_ge_i32 s98, 3\n"
    "s_cbranch_scc1 .L141_%=\n"
    "s_waitcnt vmcnt(12)\n"
    "s_cmp_ge_i32 s98, 2\n"
    "s_cbranch_scc1 .L141_%=\n"
    "s_waitcnt vmcnt(10)\n"
    "s_branch .L141_%=\n"
    ".L142_%=:\n"
    "s_cmp_lt_i32 s89, s90\n"
    "s_cbranch_scc1 .L139_%=\n"
    "s_waitcnt vmcnt(0)\n"
    "s_branch .L141_%=\n"
    ".Lwop_%=:\n"
    "s_add_u32 s44, s34, s26\n"
    "s_add_u32 s46, s34, s27\n"
    "s_waitcnt vmcnt(2) lgkmcnt(0)\n"
    "s_branch .L136_%=\n"
    ".L135_%=:\n"
    "v_max3_u32 v67, v44, v45, v46\n"
    "v_max3_u32 v67, v67, v47, v48\n"
    "v_max3_u32 v67, v67, v49, v50\n"
    "v_max3_u32 v67, v67, v51, v52\n"
    "v_max3_u32 v67, v67, v53, v54\n"
    "v_max3_u32 v67, v67, v55, v56\n"
    "v_max3_u32 v67, v67, v57, v58\n"
    "v_max3_u32 v67, v67, v59, v60\n"
    "v_cmp_eq_u32 vcc, -1, v67\n"
    "s_cbranch_vccz .L136_%=\n"
    "s_addk_i32 s41, -1\n"
    "s_cmp_gt_i32 s41, 0\n"
    "s_cbranch_scc0 .L136_%=\n"
    "s_sleep 2\n"
    "s_branch .L134_%=\n"
    ".L137_%=:\n"
    "s_cmp_lg_u32 s42, 0\n"
    "s_cbranch_scc1 .L138_%=\n"
    "s_mov_b32 s42, 1\n"
    "s_branch .L135_%=\n"
    ".L147_%=:\n"
    "s_swappc_b64 s[96:97], s[94:95]\n"
    "s_branch .L148_%=\n"
    ".L152_%=:\n"
    "buffer_load_dwordx4 v[32:35], v95, s[0:3], s44 offen offset:0 sc1\n"
    "buffer_load_dwordx4 v[36:39], v95, s[0:3], s44 offen offset:1024 sc1\n"
    "buffer_load_dwordx4 v[40:43], v95, s[0:3], s44 offen offset:2048 sc1\n"
    "buffer_load_dwordx4 v[44:47], v95, s[0:3], s44 offen offset:3072 sc1\n"
    "s_branch .L157_%=\n"
    ".L158_%=:\n"
    "s_add_i32 s98, s89, 5\n"
    "s_cmp_le_i32 s98, s90\n"
    "s_cbranch_scc0 .L162_%=\n"
    "s_waitcnt vmcnt(12)\n"
    "s_swappc_b64 s[96:97], s[94:95]\n"
    "s_waitcnt vmcnt(12)\n"
    "s_swappc_b64 s[96:97], s[94:95]\n"
    "s_waitcnt vmcnt(12)\n"
    "s_swappc_b64 s[96:97], s[94:95]\n"
    "s_waitcnt vmcnt(12)\n"
    "s_swappc_b64 s[96:97], s[94:95]\n"
    "s_waitcnt vmcnt(12)\n"
    "s_swappc_b64 s[96:97], s[94:95]\n"
    "s_waitcnt vmcnt(10)\n"
    "s_sub_i32 s98, s89, 5\n"
    "s_max_i32 s91, s91, s98\n"
    "s_branch .L160_%=\n"
    ".L162_%=:\n"
    "s_mov_b32 s99, s89\n"
    ".L163_%=:\n"
    "s_swappc_b64 s[96:97], s[94:95]\n"
    "s_cmp_lt_i32 s89, s90\n"
    "s_cbranch_scc1 .L163_%=\n"
    "s_max_i32 s91, s91, s99\n"
    "s_sub_i32 s98, s89, s99\n"
    "s_waitcnt vmcnt(8)\n"
    "s_cmp_ge_i32 s98, 4\n"
    "s_cbranch_scc1 .L160_%=\n"
    "s_waitcnt vmcnt(6)\n"
    "s_cmp_ge_i32 s98, 3\n"
    "s_cbranch_scc1 .L160_%=\n"
    "s_waitcnt vmcnt(4)\n"
    "s_cmp_ge_i32 s98, 2\n"
    "s_cbranch_scc1 .L160_%=\n"
    "s_waitcnt vmcnt(2)\n"
    "s_branch .L160_%=\n"
    ".L159_%=:\n"
    "s_add_i32 s98, s89, 5\n"
    "s_cmp_le_i32 s98, s90\n"
    "s_cbranch_scc0 .L164_%=\n"
    "s_waitcnt vmcnt(20)\n"
    "s_swappc_b64 s[96:97], s[94:95]\n"
    "s_waitcnt vmcnt(20)\n"
    "s_swappc_b64 s[96:97], s[94:95]\n"
    "s_waitcnt vmcnt(20)\n"
    "s_swappc_b64 s[96:97], s[94:95]\n"
    "s_waitcnt vmcnt(20)\n"
    "s_swappc_b64 s[96:97], s[94:95]\n"
    "s_waitcnt vmcnt(20)\n"
    "s_swappc_b64 s[96:97], s[94:95]\n"
    "s_waitcnt vmcnt(18)\n"
    "s_sub_i32 s98, s89, 5\n"
    "s_max_i32 s91, s91, s98\n"
    "s_branch .L160_%=\n"
    ".L164_%=:\n"
    "s_mov_b32 s99, s89\n"
    ".L165_%=:\n"
    "s_swappc_b64 s[96:97], s[94:95]\n"
    "s_cmp_lt_i32 s89, s90\n"
    "s_cbranch_scc1 .L165_%=\n"
    "s_max_i32 s91, s91, s99\n"
    "s_sub_i32 s98, s89, s99\n"
    "s_waitcnt vmcnt(16)\n"
    "s_cmp_ge_i32 s98, 4\n"
    "s_cbranch_scc1 .L160_%=\n"
    "s_waitcnt vmcnt(14)\n"
    "s_cmp_ge_i32 s98, 3\n"
    "s_cbranch_scc1 .L160_%=\n"
    "s_waitcnt vmcnt(12)\n"
    "s_cmp_ge_i32 s98, 2\n"
    "s_cbranch_scc1 .L160_%=\n"
    "s_waitcnt vmcnt(10)\n"
    "s_branch .L160_%=\n"
    ".L161_%=:\n"
    "s_cmp_lt_i32 s89, s90\n"
    "s_cbranch_scc1 .L158_%=\n"
    "s_waitcnt vmcnt(0)\n"
    "s_branch .L160_%=\n"
    ".L153_%=:\n"
    "v_max3_u32 v79, v32, v33, v34\n"
    "v_max3_u32 v79, v79, v35, v36\n"
    "v_max3_u32 v79, v79, v37, v38\n"
    "v_max3_u32 v79, v79, v39, v40\n"
    "v_max3_u32 v79, v79, v41, v42\n"
    "v_max3_u32 v79, v79, v43, v44\n"
    "v_max3_u32 v79, v79, v45, v46\n"
    "v_max_u32 v79, v79, v47\n"
    "v_cmp_eq_u32 vcc, -1, v79\n"
    "s_cbranch_vccz .L154_%=\n"
    "s_addk_i32 s41, -1\n"
    "s_cmp_gt_i32 s41, 0\n"
    "s_cbranch_scc0 .L154_%=\n"
    "s_sleep 2\n"
    "s_branch .L152_%=\n"
    ".L155_%=:\n"
    "s_cmp_lg_u32 s42, 0\n"
    "s_cbranch_scc1 .L156_%=\n"
    "s_mov_b32 s42, 1\n"
    "s_addk_i32 s41, -1\n"
    "s_cmp_gt_i32 s41, 0\n"
    "s_cbranch_scc0 .L156_%=\n"
    "s_sleep 2\n"
    "s_branch .L152_%=\n"
    ".L166_%=:\n"
    "s_setpc_b64 s[56:57]\n"
    ".L168_%=:\n"
    "s_swappc_b64 s[96:97], s[94:95]\n"
    "s_branch .L169_%=\n"
    ".L173_%=:\n"
    "s_cmp_lt_i32 s89, s90\n"
    "s_cbranch_scc0 .L175_%=\n"
    "s_waitcnt vmcnt(8)\n"
    "s_sub_i32 s98, s89, 4\n"
    "s_max_i32 s91, s91, s98\n"
    "s_swappc_b64 s[96:97], s[94:95]\n"
    "s_cmp_lt_i32 s91, s44\n"
    "s_cbranch_scc1 .L173_%=\n"
    "s_branch .L174_%=\n"
    ".L175_%=:\n"
    "s_waitcnt vmcnt(8)\n"
    "s_sub_i32 s98, s89, 4\n"
    "s_max_i32 s91, s91, s98\n"
    "s_cmp_ge_i32 s91, s44\n"
    "s_cbranch_scc1 .L174_%=\n"
    "s_waitcnt vmcnt(0)\n"
    "s_mov_b32 s91, s89\n"
    "s_branch .L174_%=\n"
    ".L176_%=:\n"
    "s_add_i32 s98, s89, 5\n"
    "s_cmp_le_i32 s98, s90\n"
    "s_cbranch_scc0 .L178_%=\n"
    "s_waitcnt vmcnt(10)\n"
    "s_swappc_b64 s[96:97], s[94:95]\n"
    "s_waitcnt vmcnt(10)\n"
    "s_swappc_b64 s[96:97], s[94:95]\n"
    "s_waitcnt vmcnt(10)\n"
    "s_swappc_b64 s[96:97], s[94:95]\n"
    "s_waitcnt vmcnt(10)\n"
    "s_swappc_b64 s[96:97], s[94:95]\n"
    "s_waitcnt vmcnt(10)\n"
    "s_swappc_b64 s[96:97], s[94:95]\n"
    "s_waitcnt vmcnt(10)\n"
    "s_sub_i32 s98, s89, 5\n"
    "s_max_i32 s91, s91, s98\n"
    "s_branch .L177_%=\n"
    ".L178_%=:\n"
    "s_mov_b32 s99, s89\n"
    ".L179_%=:\n"
    "s_swappc_b64 s[96:97], s[94:95]\n"
    "s_cmp_lt_i32 s89, s90\n"
    "s_cbranch_scc1 .L179_%=\n"
    "s_max_i32 s91, s91, s99\n"
    "s_sub_i32 s98, s89, s99\n"
    "s_waitcnt vmcnt(8)\n"
    "s_cmp_ge_i32 s98, 4\n"
    "s_cbranch_scc1 .L177_%=\n"
    "s_waitcnt vmcnt(6)\n"
    "s_cmp_ge_i32 s98, 3\n"
    "s_cbranch_scc1 .L177_%=\n"
    "s_waitcnt vmcnt(4)\n"
    "s_cmp_ge_i32 s98, 2\n"
    "s_cbranch_scc1 .L177_%=\n"
    "s_waitcnt vmcnt(2)\n"
    "s_branch .L177_%=\n"
    ".L180_%=:\n"
    "s_cmp_lt_i32 s89, s90\n"
    "s_cbranch_scc0 .L182_%=\n"
    "s_waitcnt vmcnt(8)\n"
    "s_sub_i32 s98, s89, 4\n"
    "s_max_i32 s91, s91, s98\n"
    "s_swappc_b64 s[96:97], s[94:95]\n"
    "s_cmp_lt_i32 s91, s44\n"
    "s_cbranch_scc1 .L180_%=\n"
    "s_branch .L181_%=\n"
    ".L182_%=:\n"
    "s_waitcnt vmcnt(8)\n"
    "s_sub_i32 s98, s89, 4\n"
    "s_max_i32 s91, s91, s98\n"
    "s_cmp_ge_i32 s91, s44\n"
    "s_cbranch_scc1 .L181_%=\n"
    "s_waitcnt vmcnt(0)\n"
    "s_mov_b32 s91, s89\n"
    "s_branch .L181_%=\n"
    ".Lend_%=:\n"
    "s_waitcnt vmcnt(0) lgkmcnt(0)\n"
    ".Lgqa_end:\n"
    :
    : "{s[0:1]}"(p.ws), "{s[8:9]}"(p.x), "{s[10:11]}"(p.out), "{s[12:13]}"(p.wq[0]), "{s[14:15]}"(p.wq[1]), "{s[16:17]}"(p.wo[0]), "{s[18:19]}"(p.wo[1]),
      "{s20}"(c), "{s21}"(w),
      "{s22}"(196608u + RF(kva * 2048u) + bq * 64), "{s23}"(0u + RF((kva * 4 + w) * 4096u) + row * 64),
      "{s24}"(131072u + RF((kva * 4 + w) * 1024u) + row * 64), "{s25}"(180224u + (kvh * 4 + w) * 128),
      "{s26}"(0u + RF((kvh * 4 + w) * 4096u)), "{s27}"(131072u + RF((kvh * 4 + w) * 1024u)),
      "{s28}"(163840u + RF((jj >> 2) * 4096u) + (kvh >> 2) * 2048 + ((jj & 3) >> 1) * 1024 + (kvh & 3) * 256 + (jj & 1) * 128),
      "{s29}"(163840u + RF(w * 4096u)), "{s30}"(196608u + RF(kvh * 2048u) + jj * 128),
      "{s31}"((w << 20) + (kvh * 512 + jj * 32) * 2), "{s32}"(((kvh * 512 + w * 128) << 10) + jj * 64),
      "{v81}"(vQ | (vMW << 16)), "{v82}"(vMLW | (vMR << 16)), "{v83}"(vPA | (vWo << 16)), "{v84}"(vA | (vL << 16)),
      "{v85}"(vPB | (vWq << 16)), "{v86}"(vRQW | (vX << 16)), "{v87}"(vR2W | (vRQR << 16)), "{v80}"(dWo | dWq)
    : "v255", "a255", "s101", "vcc", "scc", "memory");
#endif
}

extern "C" void kernel_launch(void* const* d_in, const int* in_sizes, int n_in, void* d_out, int out_size, void* d_ws, size_t ws_size,
                              hipStream_t stream) {
  Params p;
  p.x = d_in[0];
  p.kv[0] = d_in[1]; p.kv[1] = d_in[2]; p.kv[2] = d_in[6]; p.kv[3] = d_in[7];
  p.wq[0] = d_in[3]; p.wq[1] = d_in[8];
  p.wo[0] = d_in[4]; p.wo[1] = d_in[9];
  p.out = d_out; p.ws = d_ws;
  (void)hipFuncSetAttribute((const void*)gqa4_kernel, hipFuncAttributeMaxDynamicSharedMemorySize, LDS_BYTES);
  hipLaunchKernelGGL(gqa4_kernel, dim3(256), dim3(256), LDS_BYTES, stream, p);
}
```
